```python
import torch

def _softmax(x, axis=-1):
    return torch.softmax(x, dim=axis)
def _gelu(x):
    return torch.nn.functional.gelu(x, approximate="tanh")
def _silu(x):
    return torch.nn.functional.silu(x)
def _sigmoid(x):
    return torch.sigmoid(x)
def _layernorm(x, axis=-1, eps=1e-5):
    m = x.mean(dim=axis, keepdim=True)
    v = x.var(dim=axis, keepdim=True, unbiased=False)
    return (x - m) / torch.sqrt(v + eps)
def _scatter_add(tgt, idx, src, axis=0):
    return tgt.clone().index_add_(axis, idx, src)
def _segment_sum(src, seg, n):
    out = torch.zeros((n,) + src.shape[1:], dtype=src.dtype)
    return out.index_add_(0, seg, src)
def _pad(x, pw):
    flat = tuple(v for pair in reversed(pw) for v in pair)
    return torch.nn.functional.pad(x, flat)

kernel_name = 'gated_mix__ssm_conv_s5__glu_gated_swiglu__prompt'

def setup_inputs():

    _w = (lambda a: (lambda t: t.to(torch.bfloat16) if t.is_floating_point() else t)(torch.as_tensor(a)))
    def _A_setup():
        B, S, D = 1, 2048, 1024
        P = D
        Lambda_re = -0.5 * (lambda _x: torch.exp(torch.as_tensor(_x)))((lambda *s: torch.randn(*s, dtype=torch.bfloat16) * 0.2)(P))
        Lambda_im = (lambda *s: torch.randn(*s, dtype=torch.bfloat16) * 0.4)(P) * 1.0
        B_re = (lambda *s: torch.randn(*s, dtype=torch.bfloat16) * 0.2)(P, D) * (D ** -0.5)
        B_im = (lambda *s: torch.randn(*s, dtype=torch.bfloat16) * 0.2)(P, D) * (D ** -0.5)
        C_re = (lambda *s: torch.randn(*s, dtype=torch.bfloat16) * 0.2)(D, P) * (P ** -0.5)
        C_im = (lambda *s: torch.randn(*s, dtype=torch.bfloat16) * 0.2)(D, P) * (P ** -0.5)
        log_dt = (lambda *s: torch.randn(*s, dtype=torch.bfloat16) * 0.2)(P) * 0.3 - 2.0
        Dskip = (lambda *s: torch.randn(*s, dtype=torch.bfloat16) * 0.05)(D)
        return {
            "x": _w((lambda *s: torch.randn(*s, dtype=torch.bfloat16) * 0.05)(B, S, D)),
            "Lambda_re": _w(Lambda_re),
            "Lambda_im": _w(Lambda_im),
            "B_re": _w(B_re),
            "B_im": _w(B_im),
            "C_re": _w(C_re),
            "C_im": _w(C_im),
            "log_dt": _w(log_dt),
            "Dskip": _w(Dskip),
        }
    _A_in = _A_setup()

    def _B_setup():
        B, S, D, F = 1, 2048, 1024, 3584
        return {
            "x": _w((lambda *s: torch.randn(*s, dtype=torch.bfloat16) * 0.05)(B, S, D)),
            "w1": _w((lambda *s: torch.randn(*s, dtype=torch.bfloat16) * 0.8)(D, F) * (2.0 / D) ** 0.5),
            "w3": _w((lambda *s: torch.randn(*s, dtype=torch.bfloat16) * 0.8)(D, F) * (2.0 / D) ** 0.5),
            "w2": _w((lambda *s: torch.randn(*s, dtype=torch.bfloat16) * 0.8)(F, D) * (2.0 / F) ** 0.5),
        }
    _B_in = _B_setup()

    return {
        "A_x": _A_in["x"],
        "A_Lambda_re": _A_in["Lambda_re"],
        "A_Lambda_im": _A_in["Lambda_im"],
        "A_B_re": _A_in["B_re"],
        "A_B_im": _A_in["B_im"],
        "A_C_re": _A_in["C_re"],
        "A_C_im": _A_in["C_im"],
        "A_log_dt": _A_in["log_dt"],
        "A_Dskip": _A_in["Dskip"],
        "B_x": _B_in["x"],
        "B_w1": _B_in["w1"],
        "B_w3": _B_in["w3"],
        "B_w2": _B_in["w2"],
        "gate_w": _w((lambda *s: torch.randn(*s, dtype=torch.bfloat16) * 0.05)(1024) * 16.0),
    }

def reference(A_x, A_Lambda_re, A_Lambda_im, A_B_re, A_B_im, A_C_re, A_C_im, A_log_dt, A_Dskip, B_x, B_w1, B_w3, B_w2, gate_w):
    def _A_ref(x, Lambda_re, Lambda_im, B_re, B_im, C_re, C_im, log_dt, Dskip):
        Bsz, L, H = x.shape
        Lam = Lambda_re + 1j * Lambda_im
        Bmat = B_re + 1j * B_im
        Cmat = C_re + 1j * C_im
        dt = torch.exp(log_dt)
        Lam_bar = torch.exp(dt * Lam)
        B_bar = ((Lam_bar - 1.0) / Lam)[:, None] * Bmat
        Bu = torch.einsum("bld,pd->blp", x + 0j, B_bar)
        h = torch.zeros_like(Bu[:, 0, :])
        hs = []
        for t in range(L):
            h = Lam_bar[None, :] * h + Bu[:, t, :]
            hs.append(h)
        hseq = torch.stack(hs, 1)
        y = 2.0 * torch.einsum("blp,dp->bld", hseq, Cmat).real + x * Dskip[None, None, :]
        return y

    def _B_ref(x, w1, w3, w2):
        gate = torch.einsum("bsd,df->bsf", x, w1)
        up = torch.einsum("bsd,df->bsf", x, w3)
        h = _silu(gate) * up
        out = torch.einsum("bsf,fd->bsd", h, w2)
        return out

    x = A_x
    g = _sigmoid((x * gate_w).sum(-1, keepdims=True))
    a = _A_ref(x, A_Lambda_re, A_Lambda_im, A_B_re, A_B_im, A_C_re, A_C_im, A_log_dt, A_Dskip)
    b = _B_ref(x, B_w1, B_w3, B_w2)
    return g * a + (1 - g) * b
```

```cpp
#include <hip/hip_runtime.h>
#include <hip/hip_bf16.h>
#include <stdint.h>

typedef unsigned short u16;
typedef unsigned long long u64;
typedef short bf16x8 __attribute__((ext_vector_type(8)));
typedef short s16x4 __attribute__((ext_vector_type(4)));
typedef float f32x4 __attribute__((ext_vector_type(4)));
typedef float f32x2 __attribute__((ext_vector_type(2)));
typedef unsigned u32x4 __attribute__((ext_vector_type(4)));
typedef unsigned u32x2 __attribute__((ext_vector_type(2)));
#define LAS __attribute__((address_space(3)))
#define GAS __attribute__((address_space(1)))
#define RLX_AGENT __ATOMIC_RELAXED, __HIP_MEMORY_SCOPE_AGENT

constexpr int S_ = 2048, D_ = 1024, P_ = 1024, F_ = 3584, KT_ = 2 * P_ + F_;

constexpr size_t WS_GRAN = 0;
constexpr size_t WS_FLAG = 131072;
constexpr size_t WS_G    = WS_FLAG + 65536;
constexpr size_t WS_TAB  = WS_G + 8192;
constexpr size_t WS_A2   = WS_TAB + 16384;
constexpr size_t WS_SLAB = WS_A2 + (size_t)S_ * KT_ * 2;

__device__ __forceinline__ float bf2f(unsigned v16) { return __uint_as_float(v16 << 16); }
__device__ __forceinline__ unsigned cvt_pk_bf16(float lo, float hi) {
  unsigned r; asm("v_cvt_pk_bf16_f32 %0, %1, %2" : "=v"(r) : "v"(lo), "v"(hi)); return r;
}
__device__ __forceinline__ float rbf(float f) { return __uint_as_float(cvt_pk_bf16(f, f) << 16); }

__global__ __launch_bounds__(512) void k_prep(const u16* __restrict__ x, const u16* __restrict__ gw,
                                              const u16* __restrict__ lre, const u16* __restrict__ lim,
                                              const u16* __restrict__ ldt, char* __restrict__ ws) {
  const int tid = threadIdx.x, wid = tid >> 6, lane = tid & 63, b = blockIdx.x;
  if (tid < 64) ((u64*)(ws + WS_GRAN))[b * 64 + tid] = 0ull;
  if (tid == 64) ((unsigned*)(ws + WS_FLAG))[b * 64] = 0u;
  const int row = b * 8 + wid;
  const u32x4* xp = (const u32x4*)(x + (size_t)row * D_);
  const u32x4* gp = (const u32x4*)gw;
  float s = 0.f;
#pragma unroll
  for (int i = 0; i < 2; ++i) {
    u32x4 a = xp[lane + 64 * i], c = gp[lane + 64 * i];
#pragma unroll
    for (int j = 0; j < 4; ++j) {
      s += rbf(bf2f(a[j] & 0xffffu) * bf2f(c[j] & 0xffffu));
      s += rbf(bf2f(a[j] >> 16) * bf2f(c[j] >> 16));
    }
  }
#pragma unroll
  for (int o = 32; o >= 1; o >>= 1) s += __shfl_xor(s, o);
  if (lane == 0) {
    s = rbf(s);
    float g = rbf(1.0f / (1.0f + expf(-s)));
    ((float*)(ws + WS_G))[row] = g;
  }
  if (b < 2) {
    const int p = b * 512 + tid;
    const float lr = bf2f(lre[p]), li = bf2f(lim[p]);
    const float dt = rbf(expf(bf2f(ldt[p])));
    const float zr = dt * lr, zi = dt * li;
    const float e = expf(zr);
    float sn, cs; sincosf(zi, &sn, &cs);
    const float br = e * cs, bi = e * sn;
    const float nr = br - 1.0f, ni = bi;
    const float den = lr * lr + li * li;
    const float cr = (nr * lr + ni * li) / den, ci = (ni * lr - nr * li) / den;
    ((f32x4*)(ws + WS_TAB))[p] = (f32x4){br, bi, cr, ci};
  }
}

constexpr int G1_STAGE = 73728;
constexpr int G1_WG = 32768, G1_WU = 49152, G1_BS = 65536;
constexpr int G1_LDS = 2 * G1_STAGE;

typedef __amdgpu_buffer_rsrc_t rsrc_t;
__device__ __forceinline__ rsrc_t mk_rsrc(const void* p, unsigned bytes) {
  return __builtin_amdgcn_make_buffer_rsrc((void*)p, 0, bytes, 0x00020000);
}
__device__ __forceinline__ void dma16(rsrc_t rs, LAS char* dst, unsigned voff, unsigned soff) {
  asm volatile("s_mov_b32 m0, %0\n\ts_nop 0\n\tbuffer_load_dwordx4 %1, %2, %3 offen lds"
               :: "s"((unsigned)(size_t)dst), "v"(voff), "s"(rs), "s"(soff) : "memory");
}
__device__ __forceinline__ bf16x8 lds_b128(LAS char* p) { return *(LAS bf16x8*)p; }
__device__ __forceinline__ bf16x8 lds_tr2(LAS char* p0, LAS char* p1) {
  s16x4 a = __builtin_amdgcn_ds_read_tr16_b64_v4i16((LAS s16x4*)p0);
  s16x4 b = __builtin_amdgcn_ds_read_tr16_b64_v4i16((LAS s16x4*)p1);
  return __builtin_shufflevector(a, b, 0, 1, 2, 3, 4, 5, 6, 7);
}
#define MFMA(a, b, c) __builtin_amdgcn_mfma_f32_16x16x32_bf16((a), (b), (c), 0, 0, 0)
#define MFMA_IP(c, a, b) asm volatile("v_mfma_f32_16x16x32_bf16 %0, %1, %2, %0" : "+v"(c) : "v"(a), "v"(b))

template <int WN>
__device__ __forceinline__ void gemm1_body(LAS char* lds, rsrc_t rx, rsrc_t rg, rsrc_t ru, rsrc_t rb_,
                                           unsigned voA, unsigned voW, unsigned voW2, int wid, int wm, int lane,
                                           f32x4 (&acc)[4][9]) {
  constexpr int NG = WN == 0 ? 4 : 3;
  constexpr int NB0 = WN == 0 ? 0 : 4;
  constexpr int NS = WN == 0 ? 1 : 3;
  constexpr int S0 = WN == 0 ? 0 : 1;
  const int fr = lane & 15, fq = lane >> 4;
  const int a_off = (wm * 64 + fr) * 128 + ((fq ^ (fr >> 1)) << 4);
  const int s_off = G1_BS + (S0 * 16 + fr) * 128 + ((fq ^ (fr >> 1)) << 4);
  const int q = (lane & 15) >> 2, p = lane & 3;
  const int w_off0 = G1_WG + (fq * 8 + NB0) * 256 + ((q + 4 * (fq & 1)) << 5) + p * 8;
  const int w_off1 = G1_WG + (fq * 8 + NB0) * 256 + ((q + 4 * (1 - (fq & 1))) << 5) + p * 8;

  auto stageA = [&](int buf, int kt) {
    LAS char* d = lds + buf * G1_STAGE;
#pragma unroll
    for (int i = 0; i < 4; ++i) dma16(rx, d + (wid + 8 * i) * 1024, voA, kt * 128 + i * 131072);
    dma16(rb_, d + G1_BS + wid * 1024, voA, kt * 128);
  };
  auto stageW = [&](int buf, int kt) {
    LAS char* d = lds + buf * G1_STAGE;
    dma16(rg, d + G1_WG + wid * 2048, voW, kt * (64 * F_ * 2));
    dma16(rg, d + G1_WG + wid * 2048 + 1024, voW2, kt * (64 * F_ * 2));
    dma16(ru, d + G1_WU + wid * 2048, voW, kt * (64 * F_ * 2));
    dma16(ru, d + G1_WU + wid * 2048 + 1024, voW2, kt * (64 * F_ * 2));
  };
#define SCHED() __builtin_amdgcn_sched_barrier(0)
#define BAR() do { SCHED(); __builtin_amdgcn_s_barrier(); SCHED(); } while (0)
  bf16x8 xa[4], fb[5];
  auto tile = [&](int buf, int ktn, bool more) {
    LAS char* base = lds + buf * G1_STAGE;
#pragma unroll
    for (int s = 0; s < 2; ++s) {
#pragma unroll
      for (int m = 0; m < 4; ++m) xa[m] = lds_b128(base + ((a_off + m * 2048) ^ (s * 64)));
#pragma unroll
      for (int i = 0; i < 2; ++i) {
        fb[2 * i] = lds_tr2(base + w_off0 + s * 8192 + i * 256, base + w_off1 + s * 8192 + i * 256);
        fb[2 * i + 1] = lds_tr2(base + w_off0 + s * 8192 + i * 256 + (G1_WU - G1_WG),
                                base + w_off1 + s * 8192 + i * 256 + (G1_WU - G1_WG));
      }
      fb[4] = lds_b128(base + (s_off ^ (s * 64)));
      if (s == 0 && more) stageA(buf ^ 1, ktn);
      asm volatile("s_waitcnt lgkmcnt(0)" ::: "memory");
      BAR();
      __builtin_amdgcn_s_setprio(1);
#pragma unroll
      for (int i = 0; i < 2; ++i) {
#pragma unroll
        for (int m = 0; m < 4; ++m) MFMA_IP(acc[m][i], fb[2 * i], xa[m]);
#pragma unroll
        for (int m = 0; m < 4; ++m) MFMA_IP(acc[m][4 + i], fb[2 * i + 1], xa[m]);
      }
#pragma unroll
      for (int m = 0; m < 4; ++m) MFMA_IP(acc[m][WN == 0 ? 8 : 3], fb[4], xa[m]);
      __builtin_amdgcn_s_setprio(0);
      BAR();
      fb[0] = lds_tr2(base + w_off0 + s * 8192 + 2 * 256, base + w_off1 + s * 8192 + 2 * 256);
      fb[1] = lds_tr2(base + w_off0 + s * 8192 + 2 * 256 + (G1_WU - G1_WG),
                      base + w_off1 + s * 8192 + 2 * 256 + (G1_WU - G1_WG));
      if (WN == 0) {
        fb[2] = lds_tr2(base + w_off0 + s * 8192 + 3 * 256, base + w_off1 + s * 8192 + 3 * 256);
        fb[3] = lds_tr2(base + w_off0 + s * 8192 + 3 * 256 + (G1_WU - G1_WG),
                        base + w_off1 + s * 8192 + 3 * 256 + (G1_WU - G1_WG));
      } else {
        fb[2] = lds_b128(base + ((s_off + 2048) ^ (s * 64)));
        fb[3] = lds_b128(base + ((s_off + 4096) ^ (s * 64)));
      }
      if (s == 0 && more) stageW(buf ^ 1, ktn);
      if (s == 1) asm volatile("s_waitcnt vmcnt(0) lgkmcnt(0)" ::: "memory");
      else asm volatile("s_waitcnt lgkmcnt(0)" ::: "memory");
      BAR();
      __builtin_amdgcn_s_setprio(1);
#pragma unroll
      for (int m = 0; m < 4; ++m) MFMA_IP(acc[m][2], fb[0], xa[m]);
#pragma unroll
      for (int m = 0; m < 4; ++m) MFMA_IP(acc[m][6], fb[1], xa[m]);
#pragma unroll
      for (int m = 0; m < 4; ++m) MFMA_IP(acc[m][WN == 0 ? 3 : 7], fb[2], xa[m]);
#pragma unroll
      for (int m = 0; m < 4; ++m) MFMA_IP(acc[m][WN == 0 ? 7 : 8], fb[3], xa[m]);
      __builtin_amdgcn_s_setprio(0);
      BAR();
    }
  };

  stageA(0, 0); stageW(0, 0);
  asm volatile("s_waitcnt vmcnt(0)" ::: "memory");
  BAR();
  if (WN == 1) BAR();
#pragma unroll 1
  for (int kt = 0; kt < 16; kt += 2) {
    tile(0, kt + 1, true);
    tile(1, kt + 2, kt + 2 < 16);
  }
  if (WN == 0) BAR();
  asm volatile("s_nop 7\n\ts_nop 7" ::: "memory");
  SCHED();
}

__global__ __launch_bounds__(512, 2) void k_gemm1(const u16* __restrict__ x, const u16* __restrict__ w1,
                                                  const u16* __restrict__ w3, const u16* __restrict__ bre,
                                                  const u16* __restrict__ bim, char* __restrict__ ws) {
  extern __shared__ __attribute__((aligned(16))) char shm[];
  LAS char* lds = (LAS char*)shm;
  const int tid = threadIdx.x, lane = tid & 63;
  const int wid = __builtin_amdgcn_readfirstlane(tid >> 6);
  const int wm = wid & 3, wn = wid >> 2;
  const int bid = blockIdx.x;
  const int cb = (bid & 7) * 4 + ((bid >> 3) & 3), rb = bid >> 5;

  const int arow = 8 * wid + (lane >> 3);
  const unsigned voA = (unsigned)(arow * D_ + (((lane & 7) ^ ((arow >> 1) & 7)) * 8)) * 2u;
  const int wk = 8 * wid + (((lane >> 1) & 7) ^ ((wid & 1) << 2));
  const unsigned voW = (unsigned)(wk * F_ + (lane >> 4) * 16 + (lane & 1) * 8) * 2u;
  const unsigned voW2 = lane < 48 ? voW + 128u : 0x80000000u;
  const rsrc_t rx = mk_rsrc(x + (size_t)rb * 256 * D_, 256u * D_ * 2u);
  const rsrc_t rg = mk_rsrc(w1 + cb * 112, (unsigned)(D_ * F_ - cb * 112) * 2u);
  const rsrc_t ru = mk_rsrc(w3 + cb * 112, (unsigned)(D_ * F_ - cb * 112) * 2u);
  const rsrc_t rbs = wid < 4 ? mk_rsrc(bre + (size_t)cb * 32 * D_, 32u * D_ * 2u)
                             : mk_rsrc(bim + (size_t)cb * 32 * D_ - 32 * D_, 64u * D_ * 2u);

  f32x4 acc[4][9];
#pragma unroll
  for (int m = 0; m < 4; ++m)
#pragma unroll
    for (int i = 0; i < 9; ++i) acc[m][i] = (f32x4){0, 0, 0, 0};
  if (wn == 0) gemm1_body<0>(lds, rx, rg, ru, rbs, voA, voW, voW2, wid, wm, lane, acc);
  else         gemm1_body<1>(lds, rx, rg, ru, rbs, voA, voW, voW2, wid, wm, lane, acc);

  const int fr = lane & 15, fq = lane >> 4;
  LAS float* U = (LAS float*)lds;
  LAS f32x2* AGG = (LAS f32x2*)(lds + G1_STAGE);
  LAS float* SP = (LAS float*)(lds + G1_STAGE + 4096);
#pragma unroll
  for (int m = 0; m < 4; ++m) {
    const int t = wm * 64 + m * 16 + fr;
    if (wn == 0) {
      *(LAS f32x4*)(U + t * 68 + 4 * fq) = acc[m][8];
    } else {
      *(LAS f32x4*)(U + t * 68 + 16 + 4 * fq) = acc[m][3];
      *(LAS f32x4*)(U + t * 68 + 32 + 4 * fq) = acc[m][7];
      *(LAS f32x4*)(U + t * 68 + 48 + 4 * fq) = acc[m][8];
    }
  }
  asm volatile("s_waitcnt lgkmcnt(0)" ::: "memory");
  __builtin_amdgcn_s_barrier();
  asm volatile("" ::: "memory");
  const int p = tid & 31, seg = tid >> 5;
  const f32x4 tb = ((const f32x4*)(ws + WS_TAB))[cb * 32 + p];
  const float lr = tb[0], li = tb[1];
  float sr[16], si[16];
#pragma unroll
  for (int i = 0; i < 16; ++i) { sr[i] = U[(seg * 16 + i) * 68 + p]; si[i] = U[(seg * 16 + i) * 68 + 32 + p]; }
#pragma unroll
  for (int i = 1; i < 16; ++i) {
    const float a = sr[i - 1], b = si[i - 1];
    sr[i] += lr * a - li * b;
    si[i] += lr * b + li * a;
  }
  AGG[seg * 32 + p] = (f32x2){sr[15], si[15]};
  float l16r = lr, l16i = li;
#pragma unroll
  for (int i = 0; i < 4; ++i) { const float a = l16r * l16r - l16i * l16i, b = 2.f * l16r * l16i; l16r = a; l16i = b; }
  float l256r = l16r, l256i = l16i;
#pragma unroll
  for (int i = 0; i < 4; ++i) { const float a = l256r * l256r - l256i * l256i, b = 2.f * l256r * l256i; l256r = a; l256i = b; }
  asm volatile("s_waitcnt lgkmcnt(0)" ::: "memory");
  __builtin_amdgcn_s_barrier();
  asm volatile("" ::: "memory");
  float cr = 0.f, ci = 0.f, lsr = 1.f, lsi = 0.f;
  for (int s2 = 0; s2 < seg; ++s2) {
    const f32x2 a = AGG[s2 * 32 + p];
    const float nr = l16r * cr - l16i * ci + a[0], ni = l16r * ci + l16i * cr + a[1];
    cr = nr; ci = ni;
    const float mr = lsr * l16r - lsi * l16i, mi = lsr * l16i + lsi * l16r;
    lsr = mr; lsi = mi;
  }
  GAS u64* gran = (GAS u64*)(ws + WS_GRAN) + (size_t)cb * 8 * 64;
  if (seg == 15 && rb < 7) {
    const float tr_ = l16r * cr - l16i * ci + sr[15], ti_ = l16r * ci + l16i * cr + si[15];
    __hip_atomic_store(gran + rb * 64 + p, (1ull << 32) | (u64)__float_as_uint(tr_), RLX_AGENT);
    __hip_atomic_store(gran + rb * 64 + 32 + p, (1ull << 32) | (u64)__float_as_uint(ti_), RLX_AGENT);
  }
  u16* A2 = (u16*)(ws + WS_A2);
  const float* gv = (const float*)(ws + WS_G);
  {
    const int nb0 = wn * 4, ng = wn == 0 ? 4 : 3;
#pragma unroll
    for (int m = 0; m < 4; ++m) {
      const int t = rb * 256 + wm * 64 + m * 16 + fr;
      const float omg = 1.0f - gv[t];
      u16* dst = A2 + (size_t)t * KT_ + 2 * P_ + cb * 112 + nb0 * 16 + 4 * fq;
#pragma unroll
      for (int i = 0; i < 4; ++i) {
        if (i < ng) {
          float h[4];
#pragma unroll
          for (int j = 0; j < 4; ++j) {
            const float a = acc[m][i][j];
            h[j] = a * __builtin_amdgcn_rcpf(1.0f + __expf(-a)) * acc[m][4 + i][j] * omg;
          }
          *(u32x2*)(dst + i * 16) = (u32x2){cvt_pk_bf16(h[0], h[1]), cvt_pk_bf16(h[2], h[3])};
        }
      }
    }
  }
  if (tid < rb * 64) {
    u64 v; unsigned spins = 0;
    for (;;) {
      v = __hip_atomic_load(gran + tid, RLX_AGENT);
      if ((v >> 32) == 1ull) break;
      if (++spins > (1u << 24)) break;
      __builtin_amdgcn_s_sleep(1);
    }
    SP[tid] = __uint_as_float((unsigned)v);
  }
  asm volatile("s_waitcnt lgkmcnt(0)" ::: "memory");
  __builtin_amdgcn_s_barrier();
  asm volatile("" ::: "memory");
  float hr = 0.f, hi = 0.f;
  for (int c = 0; c < rb; ++c) {
    const float a = SP[c * 64 + p], b = SP[c * 64 + 32 + p];
    const float nr = l256r * hr - l256i * hi + a, ni = l256r * hi + l256i * hr + b;
    hr = nr; hi = ni;
  }
  cr += lsr * hr - lsi * hi;
  ci += lsr * hi + lsi * hr;
  LAS u16* HS = (LAS u16*)lds;
  const float cfr = tb[2], cfi = tb[3];
#pragma unroll
  for (int i = 0; i < 16; ++i) {
    const float nr = lr * cr - li * ci, ni = lr * ci + li * cr;
    cr = nr; ci = ni;
    const float a = sr[i] + cr, b = si[i] + ci;
    const float g2 = 2.0f * gv[rb * 256 + seg * 16 + i];
    const float orr = (cfr * a - cfi * b) * g2, oi = -(cfr * b + cfi * a) * g2;
    const unsigned pk = cvt_pk_bf16(orr, oi);
    HS[(seg * 16 + i) * 64 + p] = (u16)(pk & 0xffffu);
    HS[(seg * 16 + i) * 64 + 32 + p] = (u16)(pk >> 16);
  }
  asm volatile("s_waitcnt lgkmcnt(0)" ::: "memory");
  __builtin_amdgcn_s_barrier();
  asm volatile("" ::: "memory");
#pragma unroll
  for (int i = 0; i < 4; ++i) {
    const int idx = tid + 512 * i, t = idx >> 3, c = idx & 7;
    const u32x4 v = *(LAS u32x4*)(lds + idx * 16);
    *(u32x4*)(A2 + (size_t)(rb * 256 + t) * KT_ + (c >> 2) * P_ + cb * 32 + (c & 3) * 8) = v;
  }
}

constexpr int G2_STAGE = 32768, G2_NBUF = 4, G2_LDS = G2_STAGE * G2_NBUF;
constexpr int G2_NKT = 44;

__global__ __launch_bounds__(512, 2) void k_gemm2(const u16* __restrict__ x, const u16* __restrict__ cre,
                                                  const u16* __restrict__ cim, const u16* __restrict__ w2,
                                                  const u16* __restrict__ dskip, char* __restrict__ ws,
                                                  float* __restrict__ out) {
  extern __shared__ __attribute__((aligned(16))) char shm[];
  LAS char* lds = (LAS char*)shm;
  const int tid = threadIdx.x, lane = tid & 63;
  const int wid = __builtin_amdgcn_readfirstlane(tid >> 6);
  const int kg = wid >> 2, wm = (wid >> 1) & 1, wn = wid & 1;
  const int bid = blockIdx.x, xcd = bid & 7, idx = bid >> 3;
  const int rt = (xcd >> 1) * 4 + (idx & 3), ct = (xcd & 1) * 4 + ((idx >> 2) & 3), hs = idx >> 4;
  const int fr = lane & 15, fq = lane >> 4;
  const u16* A2 = (const u16*)(ws + WS_A2);

  const int arow = 8 * wid + (lane >> 3);
  const unsigned cs8 = (unsigned)(((lane & 7) ^ ((arow >> 1) & 7)) * 8);
  const unsigned voA = (unsigned)(arow * KT_ + cs8) * 2u;
  const unsigned voC = (unsigned)(arow * P_ + cs8) * 2u;
  const int wk = 8 * wid + (((lane >> 1) & 7) ^ ((wid & 1) << 2));
  const unsigned voW = (unsigned)(wk * D_ + (lane >> 4) * 16 + (lane & 1) * 8) * 2u;
  const rsrc_t ra = mk_rsrc(A2 + (size_t)rt * 128 * KT_, 128u * KT_ * 2u);
  const rsrc_t rcr = mk_rsrc(cre + (size_t)ct * 128 * P_, 128u * P_ * 2u);
  const rsrc_t rci = mk_rsrc(cim + (size_t)ct * 128 * P_, 128u * P_ * 2u);
  const rsrc_t rw = mk_rsrc(w2 + ct * 128, (unsigned)(F_ * D_ - ct * 128) * 2u);
  const int kt0 = hs * G2_NKT;

  auto stage = [&](int buf, int ktg) {
    LAS char* d = lds + buf * G2_STAGE;
    dma16(ra, d + wid * 1024, voA, ktg * 128);
    dma16(ra, d + wid * 1024 + 8192, voA, ktg * 128 + 64 * KT_ * 2);
    if (ktg < 32) {
      const rsrc_t rc = ktg < 16 ? rcr : rci;
      dma16(rc, d + 16384 + wid * 1024, voC, (ktg & 15) * 128);
      dma16(rc, d + 16384 + wid * 1024 + 8192, voC, (ktg & 15) * 128 + 64 * P_ * 2);
    } else {
      dma16(rw, d + 16384 + wid * 2048, voW, (ktg - 32) * (64 * D_ * 2));
      dma16(rw, d + 16384 + wid * 2048 + 1024, voW, (ktg - 32) * (64 * D_ * 2) + 128);
    }
  };
  const int a_off = (wm * 64 + fr) * 128 + (((4 * kg + fq) ^ (fr >> 1)) << 4);
  const int c_off = 16384 + (wn * 64 + fr) * 128 + (((4 * kg + fq) ^ (fr >> 1)) << 4);
  const int q = (lane & 15) >> 2, p = lane & 3;
  const int w_off0 = 16384 + ((4 * kg + fq) * 8 + wn * 4) * 256 + ((q + 4 * (fq & 1)) << 5) + p * 8;
  const int w_off1 = 16384 + ((4 * kg + fq) * 8 + wn * 4) * 256 + ((q + 4 * (1 - (fq & 1))) << 5) + p * 8;

  auto rd = [&](int buf, int ktg, bf16x8 (&fa)[4], bf16x8 (&fb)[4]) {
    LAS char* base = lds + buf * G2_STAGE;
#pragma unroll
    for (int m = 0; m < 4; ++m) fa[m] = lds_b128(base + a_off + m * 2048);
    if (ktg < 32) {
#pragma unroll
      for (int n = 0; n < 4; ++n) fb[n] = lds_b128(base + c_off + n * 2048);
    } else {
#pragma unroll
      for (int n = 0; n < 4; ++n) fb[n] = lds_tr2(base + w_off0 + n * 256, base + w_off1 + n * 256);
    }
  };

  f32x4 acc[4][4];
#pragma unroll
  for (int m = 0; m < 4; ++m)
#pragma unroll
    for (int n = 0; n < 4; ++n) acc[m][n] = (f32x4){0, 0, 0, 0};
  bf16x8 fa0[4], fb0[4], fa1[4], fb1[4];

  stage(0, kt0); stage(1, kt0 + 1); stage(2, kt0 + 2); stage(3, kt0 + 3);
  asm volatile("s_waitcnt vmcnt(12)" ::: "memory");
  __builtin_amdgcn_s_barrier();
  rd(0, kt0, fa0, fb0);
#pragma unroll 1
  for (int kt = 0; kt < G2_NKT; kt += 2) {
    asm volatile("s_waitcnt vmcnt(8) lgkmcnt(0)" ::: "memory");
    __builtin_amdgcn_s_barrier();
    if (kt + 4 < G2_NKT) stage(kt & 3, kt0 + kt + 4);
    else { asm volatile("" ::: "memory"); }
    rd((kt + 1) & 3, kt0 + kt + 1, fa1, fb1);
#pragma unroll
    for (int m = 0; m < 4; ++m)
#pragma unroll
      for (int n = 0; n < 4; ++n) acc[m][n] = MFMA(fb0[n], fa0[m], acc[m][n]);
    if (kt + 4 < G2_NKT) asm volatile("s_waitcnt vmcnt(8) lgkmcnt(0)" ::: "memory");
    else asm volatile("s_waitcnt vmcnt(0) lgkmcnt(0)" ::: "memory");
    __builtin_amdgcn_s_barrier();
    if (kt + 5 < G2_NKT) stage((kt + 1) & 3, kt0 + kt + 5);
    if (kt + 2 < G2_NKT) rd((kt + 2) & 3, kt0 + kt + 2, fa0, fb0);
#pragma unroll
    for (int m = 0; m < 4; ++m)
#pragma unroll
      for (int n = 0; n < 4; ++n) acc[m][n] = MFMA(fb1[n], fa1[m], acc[m][n]);
  }
  asm volatile("s_waitcnt vmcnt(0) lgkmcnt(0)" ::: "memory");
  __builtin_amdgcn_s_barrier();
  asm volatile("" ::: "memory");
  {
    LAS f32x4* X = (LAS f32x4*)lds;
#pragma unroll
    for (int mm = 0; mm < 2; ++mm)
#pragma unroll
      for (int n = 0; n < 4; ++n) {
        const f32x4 v = kg ? acc[mm][n] : acc[2 + mm][n];
        X[(wid * 8 + mm * 4 + n) * 64 + lane] = v;
      }
    asm volatile("s_waitcnt lgkmcnt(0)" ::: "memory");
    __builtin_amdgcn_s_barrier();
    asm volatile("" ::: "memory");
  }
  f32x4 r[2][4];
  {
    LAS f32x4* X = (LAS f32x4*)lds;
    const int pw = wid ^ 4;
#pragma unroll
    for (int mm = 0; mm < 2; ++mm)
#pragma unroll
      for (int n = 0; n < 4; ++n) {
        const f32x4 own = kg ? acc[2 + mm][n] : acc[mm][n];
        r[mm][n] = own + X[(pw * 8 + mm * 4 + n) * 64 + lane];
      }
  }
  const int tile = rt * 8 + ct;
  char* slab_me = ws + WS_SLAB + (size_t)(tile * 2 + hs) * 32768;
  char* slab_pt = ws + WS_SLAB + (size_t)(tile * 2 + (hs ^ 1)) * 32768;
  GAS unsigned* flag_me = (GAS unsigned*)(ws + WS_FLAG) + (tile * 2 + hs) * 64;
  GAS unsigned* flag_pt = (GAS unsigned*)(ws + WS_FLAG) + (tile * 2 + (hs ^ 1)) * 64;
  const int sw = kg * 2 + wn;
  if (wm != hs) {
    __amdgpu_buffer_rsrc_t rs = __builtin_amdgcn_make_buffer_rsrc(slab_me, 0, 32768, 0x00020000);
#pragma unroll
    for (int mm = 0; mm < 2; ++mm)
#pragma unroll
      for (int n = 0; n < 4; ++n)
        __builtin_amdgcn_raw_buffer_store_b128(__builtin_bit_cast(u32x4, r[mm][n]), rs,
                                               ((sw * 8 + mm * 4 + n) * 64 + lane) * 16, 0, 16);
    asm volatile("s_waitcnt vmcnt(0)" ::: "memory");
  }
  asm volatile("" ::: "memory");
  __builtin_amdgcn_s_barrier();
  asm volatile("" ::: "memory");
  if (tid == 0) __hip_atomic_store(flag_me, 1u, RLX_AGENT);
  if (wid == (unsigned)(hs * 2)) {
    unsigned spins = 0;
    while ((unsigned)__builtin_amdgcn_readfirstlane(__hip_atomic_load(flag_pt, RLX_AGENT)) != 1u) {
      if (++spins > (1u << 24)) break;
      __builtin_amdgcn_s_sleep(1);
    }
  }
  asm volatile("" ::: "memory");
  __builtin_amdgcn_s_barrier();
  asm volatile("" ::: "memory");
  if (wm == hs) {
    __amdgpu_buffer_rsrc_t rs = __builtin_amdgcn_make_buffer_rsrc(slab_pt, 0, 32768, 0x00020000);
    u32x4 pv[2][4];
#pragma unroll
    for (int mm = 0; mm < 2; ++mm)
#pragma unroll
      for (int n = 0; n < 4; ++n)
        pv[mm][n] = __builtin_amdgcn_raw_buffer_load_b128(rs, ((sw * 8 + mm * 4 + n) * 64 + lane) * 16, 0, 16);
    const float* gv = (const float*)(ws + WS_G);
#pragma unroll
    for (int mm = 0; mm < 2; ++mm) {
      const int t = rt * 128 + wm * 64 + (2 * kg + mm) * 16 + fr;
      const float g = gv[t];
#pragma unroll
      for (int n = 0; n < 4; ++n) {
        const int d = ct * 128 + wn * 64 + n * 16 + 4 * fq;
        const u32x2 xv = *(const u32x2*)(x + (size_t)t * D_ + d);
        const u32x2 dv = *(const u32x2*)(dskip + d);
        f32x4 o = r[mm][n] + __builtin_bit_cast(f32x4, pv[mm][n]);
        o[0] += g * rbf(bf2f(xv[0] & 0xffffu) * bf2f(dv[0] & 0xffffu));
        o[1] += g * rbf(bf2f(xv[0] >> 16) * bf2f(dv[0] >> 16));
        o[2] += g * rbf(bf2f(xv[1] & 0xffffu) * bf2f(dv[1] & 0xffffu));
        o[3] += g * rbf(bf2f(xv[1] >> 16) * bf2f(dv[1] >> 16));
        *(f32x4*)(out + (size_t)t * D_ + d) = o;
      }
    }
  }
}

extern "C" void kernel_launch(void* const* d_in, const int* in_sizes, int n_in, void* d_out, int out_size,
                              void* d_ws, size_t ws_size, hipStream_t stream) {
  const u16* x = (const u16*)d_in[0];
  const u16* lre = (const u16*)d_in[1];
  const u16* lim = (const u16*)d_in[2];
  const u16* bre = (const u16*)d_in[3];
  const u16* bim = (const u16*)d_in[4];
  const u16* cre = (const u16*)d_in[5];
  const u16* cim = (const u16*)d_in[6];
  const u16* ldt = (const u16*)d_in[7];
  const u16* dsk = (const u16*)d_in[8];
  const u16* w1 = (const u16*)d_in[10];
  const u16* w3 = (const u16*)d_in[11];
  const u16* w2 = (const u16*)d_in[12];
  const u16* gw = (const u16*)d_in[13];
  char* ws = (char*)d_ws;
  hipFuncSetAttribute((const void*)k_gemm1, hipFuncAttributeMaxDynamicSharedMemorySize, G1_LDS);
  hipFuncSetAttribute((const void*)k_gemm2, hipFuncAttributeMaxDynamicSharedMemorySize, G2_LDS);
  k_prep<<<256, 512, 0, stream>>>(x, gw, lre, lim, ldt, ws);
  k_gemm1<<<256, 512, G1_LDS, stream>>>(x, w1, w3, bre, bim, ws);
  k_gemm2<<<256, 512, G2_LDS, stream>>>(x, cre, cim, w2, dsk, ws, (float*)d_out);
}
```

```cpp
#include <hip/hip_runtime.h>
#include <hip/hip_bf16.h>
#include <stdint.h>
#include <type_traits>

typedef unsigned short u16;
typedef unsigned long long u64;
typedef short bf16x8 __attribute__((ext_vector_type(8)));
typedef short s16x4 __attribute__((ext_vector_type(4)));
typedef float f32x4 __attribute__((ext_vector_type(4)));
typedef float f32x2 __attribute__((ext_vector_type(2)));
typedef unsigned u32x4 __attribute__((ext_vector_type(4)));
typedef unsigned u32x2 __attribute__((ext_vector_type(2)));
#define LAS __attribute__((address_space(3)))
#define GAS __attribute__((address_space(1)))
#define RLX_AGENT __ATOMIC_RELAXED, __HIP_MEMORY_SCOPE_AGENT

#define PFD 6
constexpr int S_ = 2048, D_ = 1024, P_ = 1024, F_ = 3584, KT_ = 2 * P_ + F_;

constexpr size_t WS_GRAN = 0;
constexpr size_t WS_FLAG = 131072;
constexpr size_t WS_G    = WS_FLAG + 65536;
constexpr size_t WS_TAB  = WS_G + 8192;
constexpr size_t WS_A2   = WS_TAB + 16384;
constexpr size_t WS_SLAB = WS_A2 + (size_t)S_ * KT_ * 2;

#define STAMP(ws, k) do {} while (0)
__device__ __forceinline__ float bf2f(unsigned v16) { return __uint_as_float(v16 << 16); }
__device__ __forceinline__ unsigned cvt_pk_bf16(float lo, float hi) {
  unsigned r; asm("v_cvt_pk_bf16_f32 %0, %1, %2" : "=v"(r) : "v"(lo), "v"(hi)); return r;
}
__device__ __forceinline__ float rbf(float f) { return __uint_as_float(cvt_pk_bf16(f, f) << 16); }

__global__ __launch_bounds__(512) void k_prep(const u16* __restrict__ x, const u16* __restrict__ gw,
                                              const u16* __restrict__ lre, const u16* __restrict__ lim,
                                              const u16* __restrict__ ldt, char* __restrict__ ws,
                                              const u16* __restrict__ w1, const u16* __restrict__ w3,
                                              const u16* __restrict__ bre, const u16* __restrict__ bim,
                                              const u16* __restrict__ cre, const u16* __restrict__ cim,
                                              const u16* __restrict__ w2) {
  const int tid = threadIdx.x, wid = tid >> 6, lane = tid & 63, b = blockIdx.x;
  const int pch = b * 4 + (tid & 3);
  const unsigned in_lr = lre[pch], in_li = lim[pch], in_dt = ldt[pch];
  u32x4 wacc = {0u, 0u, 0u, 0u};
  {
    const int xg = b & 7, j32 = b >> 3;
    constexpr int CPR = PFD * 4;
    constexpr int NX = (64 * CPR + 511) / 512, NW = (PFD * 2 * 56 + 511) / 512;
    u32x4 v[NX + NW + 1];
#pragma unroll
    for (int i = 0; i < NX; ++i) {
      int c = tid + i * 512; c = c < 64 * CPR ? c : 0;
      v[i] = *(const u32x4*)(x + (size_t)(64 * j32 + c / CPR) * D_ + (c % CPR) * 8);
    }
#pragma unroll
    for (int i = 0; i < NW; ++i) {
      int c = tid + i * 512; c = c < PFD * 2 * 56 ? c : 0;
      const int mat = c / (PFD * 56), r = c % (PFD * 56);
      v[NX + i] = *(const u32x4*)((mat ? w3 : w1) + (size_t)(j32 * PFD + r / 56) * F_ + xg * 448 + (r % 56) * 8);
    }
    {
      const int c = tid < 8 * CPR ? tid : 0, r = c / CPR;
      v[NX + NW] = *(const u32x4*)((r < 4 ? bre : bim) + (size_t)(128 * xg + 4 * j32 + (r & 3)) * D_ + (c % CPR) * 8);
    }
#pragma unroll
    for (int i = 0; i < NX + NW + 1; ++i) wacc ^= v[i];
  }
  if (tid < 64) ((u64*)(ws + WS_GRAN))[b * 64 + tid] = 0ull;
  if (tid == 64) ((unsigned*)(ws + WS_FLAG))[b * 64] = 0u;
  const int row = b * 8 + wid;
  const u32x4* xp = (const u32x4*)(x + (size_t)row * D_);
  const u32x4* gp = (const u32x4*)gw;
  float s = 0.f;
#pragma unroll
  for (int i = 0; i < 2; ++i) {
    u32x4 a = xp[lane + 64 * i], c = gp[lane + 64 * i];
#pragma unroll
    for (int j = 0; j < 4; ++j) {
      s += rbf(bf2f(a[j] & 0xffffu) * bf2f(c[j] & 0xffffu));
      s += rbf(bf2f(a[j] >> 16) * bf2f(c[j] >> 16));
    }
  }
#pragma unroll
  for (int o = 32; o >= 1; o >>= 1) s += __shfl_xor(s, o);
  if (lane == 0) {
    s = rbf(s);
    float g = rbf(1.0f / (1.0f + expf(-s)));
    ((float*)(ws + WS_G))[row] = g;
  }
  asm volatile("" :: "v"(wacc));
  if (tid >= 448 && tid < 452) {
    const int p = pch;
    const float lr = bf2f(in_lr), li = bf2f(in_li);
    const float dt = rbf(expf(bf2f(in_dt)));
    const float zr = dt * lr, zi = dt * li;
    const float e = expf(zr);
    float sn, cs; sincosf(zi, &sn, &cs);
    const float br = e * cs, bi = e * sn;
    const float nr = br - 1.0f, ni = bi;
    const float den = lr * lr + li * li;
    const float cr = (nr * lr + ni * li) / den, ci = (ni * lr - nr * li) / den;
    ((f32x4*)(ws + WS_TAB))[p] = (f32x4){br, bi, cr, ci};
  }
}

constexpr int G1_SLOT = 36864;
constexpr int G1_WG = 16384, G1_WU = 24576, G1_BS = 32768;
constexpr int G1_RING = 4 * G1_SLOT;
constexpr int G1_LDS = G1_RING + 8192;

typedef __amdgpu_buffer_rsrc_t rsrc_t;
__device__ __forceinline__ rsrc_t mk_rsrc(const void* p, unsigned bytes) {
  return __builtin_amdgcn_make_buffer_rsrc((void*)p, 0, bytes, 0x00020000);
}
__device__ __forceinline__ void dma16(rsrc_t rs, LAS char* dst, unsigned voff, unsigned soff) {
  asm volatile("s_mov_b32 m0, %0\n\ts_nop 0\n\tbuffer_load_dwordx4 %1, %2, %3 offen lds"
               :: "s"((unsigned)(size_t)dst), "v"(voff), "s"(rs), "s"(soff) : "memory");
}
__device__ __forceinline__ void touch4(rsrc_t rs, LAS char* dst, unsigned voff, unsigned soff) {
  soff = __builtin_amdgcn_readfirstlane(soff);
  asm volatile("" : "+s"(soff));
  asm volatile("s_mov_b32 m0, %0\n\ts_nop 0\n\tbuffer_load_dword %1, %2, %3 offen lds"
               :: "s"((unsigned)(size_t)dst), "v"(voff), "s"(rs), "s"(soff) : "memory");
}
__device__ __forceinline__ void touchg(const char* p, LAS char* dst) {
  asm volatile("s_mov_b32 m0, %0\n\ts_nop 0\n\tglobal_load_lds_dword %1, off" :: "s"((unsigned)(size_t)dst), "v"(p) : "memory");
}
__device__ __forceinline__ bf16x8 lds_b128(LAS char* p) { return *(LAS bf16x8*)p; }
__device__ __forceinline__ bf16x8 lds_tr2(LAS char* p0, LAS char* p1) {
  s16x4 a = __builtin_amdgcn_ds_read_tr16_b64_v4i16((LAS s16x4*)p0);
  s16x4 b = __builtin_amdgcn_ds_read_tr16_b64_v4i16((LAS s16x4*)p1);
  return __builtin_shufflevector(a, b, 0, 1, 2, 3, 4, 5, 6, 7);
}
#define MFMA(a, b, c) __builtin_amdgcn_mfma_f32_16x16x32_bf16((a), (b), (c), 0, 0, 0)
#define MFMA_IP(c, a, b) asm volatile("v_mfma_f32_16x16x32_bf16 %0, %1, %2, %0" : "+v"(c) : "v"(a), "v"(b))

#define A2_STORE(p, v) (*(u32x4*)(p) = (v))
#define SCHED() __builtin_amdgcn_sched_barrier(0)
#define BAR() do { SCHED(); __builtin_amdgcn_s_barrier(); SCHED(); } while (0)

struct G1Stage {
  rsrc_t rx, rw, r4;
  unsigned voA, voW, voW2;
  unsigned vo4, vo5;
  const char* pf0; const char* pf1; unsigned pfs1;
  char* wsdbg;
  const char *c0, *c1, *c2;
};

template <int ROLE>
__device__ __forceinline__ void gemm1_body(LAS char* lds, G1Stage& st, int wid, int wm, int lane, f32x4 (&acc)[4][9]) {
  constexpr int WN = ROLE == 0 ? 0 : 1;
  constexpr int NG = WN == 0 ? 4 : 3;
  constexpr int NB0 = WN == 0 ? 0 : 4;
  constexpr int S0 = WN == 0 ? 0 : 1;
  constexpr int NDMA = ROLE == 2 ? 6 : 5;
  const int fr = lane & 15, fq = lane >> 4;
  const int frag = fr * 64 + ((fq ^ ((fr >> 3) << 1)) << 4);
  const int a_off = wm * 4096 + frag;
  const int s_off = G1_BS + S0 * 1024 + frag;
  const int q = (lane & 15) >> 2, p = lane & 3;
  const int w_base = G1_WG + WN * 4096 + (8 * fq + q) * 128 + ((((q >> 1) & 1) | ((fq & 1) << 1)) << 5) + p * 8;

  auto piece = [&](int i, int slot, int j) {
    LAS char* d = lds + slot * G1_SLOT;
    if (ROLE < 3) {
      if (i == 0) dma16(st.rx, d + wid * 1024, st.voA, j * 64 + wid * 32768);
      if (i == 1) dma16(st.rx, d + wid * 1024 + 7168, st.voA, j * 64 + wid * 32768 + 7 * 32768);
      if (i == 2) dma16(st.rw, d + G1_WG + (wid >> 2) * 8192 + (wid & 3) * 1024, st.voW, j * (32 * F_ * 2));
      if (i == 3) dma16(st.rw, d + G1_WG + (wid >> 2) * 8192 + (wid & 3) * 1024 + 4096, st.voW2, j * (32 * F_ * 2));
      if (i == 4) {
        if (ROLE == 0) dma16(st.r4, d + G1_BS + wid * 1024, st.voA, j * 64);
        if (ROLE == 1) dma16(st.rx, d + (10 + wid) * 1024, st.voA, j * 64 + (10 + wid) * 32768);
        if (ROLE == 2) dma16(st.rw, d + G1_WG + 8192 + 3 * 1024, st.vo4, j * (32 * F_ * 2));
      }
      if (i == 5 && ROLE == 2) dma16(st.rw, d + G1_WG + 8192 + 3 * 1024 + 4096, st.vo5, j * (32 * F_ * 2));
    }
  };
  auto stage = [&](int slot, int j) {
#pragma unroll
    for (int i = 0; i < 6; ++i) piece(i, slot, j);
  };
  auto touch = [&](int j) {
    if (ROLE == 3) {
      touchg(st.pf0, lds + G1_RING); st.pf0 += 64;
      touchg(st.pf1, lds + G1_RING + 256); st.pf1 += st.pfs1;
    }
  };
  auto step = [&](int slot, int j) {
    LAS char* base = lds + slot * G1_SLOT;
    const bool pre = j + 3 < 32;
    const int ns = (slot + 3) & 3;
    bf16x8 xa[4], fb[9];
    if (pre) piece(0, ns, j + 3);
#pragma unroll
    for (int m = 0; m < 4; ++m) xa[m] = lds_b128(base + a_off + m * 1024);
#pragma unroll
    for (int i = 0; i < NG; ++i) {
      fb[i] = lds_tr2(base + (w_base ^ (i << 5)), base + (w_base ^ (i << 5)) + 512);
      fb[4 + i] = lds_tr2(base + (w_base ^ (i << 5)) + (G1_WU - G1_WG), base + (w_base ^ (i << 5)) + 512 + (G1_WU - G1_WG));
    }
    if (WN == 0) fb[8] = lds_b128(base + s_off);
    else { fb[3] = lds_b128(base + s_off); fb[7] = lds_b128(base + s_off + 1024); fb[8] = lds_b128(base + s_off + 2048); }
    if (ROLE == 3) {
      if (j + PFD < 32) touch(j + PFD);
      if (j >= 16 && j < 27) {
        const int jj = j - 16;
        const char* pb = jj < 2 ? st.c0 + jj * 4096 : (jj < 4 ? st.c1 + (jj - 2) * 4096 : st.c2 + (jj - 4) * 4096);
        touchg(pb + lane * 64, lds + G1_RING + 512);
      }
      asm volatile("s_waitcnt lgkmcnt(0)" ::: "memory");
    } else if (pre) {
      piece(1, ns, j + 3);
      asm volatile("s_waitcnt vmcnt(%0) lgkmcnt(0)" :: "n"(NDMA + 2) : "memory");
    } else if (j + 3 == 32) {
      asm volatile("s_waitcnt vmcnt(%0) lgkmcnt(0)" :: "n"(NDMA) : "memory");
    } else {
      asm volatile("s_waitcnt vmcnt(0) lgkmcnt(0)" ::: "memory");
    }
    BAR();
    __builtin_amdgcn_s_setprio(1);
#pragma unroll
    for (int i = 0; i < 9; ++i) {
#pragma unroll
      for (int m = 0; m < 4; ++m) MFMA_IP(acc[m][i], fb[i], xa[m]);
      if (pre) {
        if (i == 1) piece(2, ns, j + 3);
        if (i == 3) piece(3, ns, j + 3);
        if (i == 5) piece(4, ns, j + 3);
        if (i == 7) piece(5, ns, j + 3);
      }
    }
    __builtin_amdgcn_s_setprio(0);
    BAR();
  };

  stage(0, 0); stage(1, 1); stage(2, 2);
  if (ROLE == 3) {
    dma16(st.rx, lds + G1_RING + 1024, (unsigned)lane * 16u, 0u);
    dma16(st.r4, lds + G1_RING + 2048, (unsigned)lane * 16u, 0u);
  } else {
    asm volatile("s_waitcnt vmcnt(%0)" :: "n"(2 * NDMA) : "memory");
  }
  BAR();
  if (WN == 1) BAR();
#pragma unroll 1
  for (int j = 0; j < 28; j += 4) {
    step(0, j); step(1, j + 1); step(2, j + 2); step(3, j + 3);
  }
  step(0, 28); step(1, 29); step(2, 30); step(3, 31);
  if (WN == 0) BAR();
  asm volatile("s_waitcnt vmcnt(0)\n\ts_nop 7\n\ts_nop 7" ::: "memory");
  SCHED();
}

__global__ __launch_bounds__(512, 2) void k_gemm1(const u16* __restrict__ x, const u16* __restrict__ w1,
                                                  const u16* __restrict__ w3, const u16* __restrict__ bre,
                                                  const u16* __restrict__ bim, char* __restrict__ ws,
                                                  const u16* __restrict__ cre, const u16* __restrict__ cim,
                                                  const u16* __restrict__ w2) {
  extern __shared__ __attribute__((aligned(16))) char shm[];
  LAS char* lds = (LAS char*)shm;
  const int tid = threadIdx.x, lane = tid & 63;
  const int wid = __builtin_amdgcn_readfirstlane(tid >> 6);
  const int wm = wid & 3, wn = wid >> 2;
  const int bid = blockIdx.x;
  const int cb = (bid & 7) * 4 + ((bid >> 3) & 3), rb = bid >> 5;
  STAMP(ws, 0);

  G1Stage st;
  const int pr = lane >> 2;
  st.voA = (unsigned)(pr * D_ + (((lane & 3) ^ ((pr >> 3) << 1)) * 8)) * 2u;
  const int c4 = (bid >> 3) & 3, l0 = (bid & 7) * 7;
  {
    const int r = lane >> 3, pos = lane & 7;
    auto woff = [&](int kb, int half) -> unsigned {
      const int k = 8 * kb + r, f = ((k >> 1) & 1) | (((k >> 3) & 1) << 1), b = (pos >> 1) ^ f;
      int blk;
      if (half == 0) blk = (l0 + c4) * 4 + b;
      else blk = c4 < 3 ? (l0 + 4 + c4) * 4 + b : (l0 + 4 + b) * 4 + 3;
      return (half == 1 && b == 3) ? 0x80000000u : (unsigned)(k * F_ + blk * 16 + (pos & 1) * 8) * 2u;
    };
    st.voW = woff(wid & 3, 0); st.voW2 = woff(wid & 3, 1);
    st.vo4 = woff(3, 0); st.vo5 = woff(3, 1);
  }
  st.rx = mk_rsrc(x + (size_t)rb * 256 * D_, 256u * D_ * 2u);
  st.rw = mk_rsrc(wn ? w3 : w1, (unsigned)(D_ * F_) * 2u);
  st.r4 = mk_rsrc((wid < 2 ? bre : bim) + (size_t)(cb * 32 + (wid & 1) * 16) * D_, 16u * D_ * 2u);
  if (wid == 7) {
    st.rx = mk_rsrc(ws + WS_G + rb * 1024, 1024u);
    st.r4 = mk_rsrc(ws + WS_TAB + cb * 512, 512u);
  }
  {
    const int xg = bid & 7, j32 = bid >> 3;
    st.pf0 = (const char*)(x + (size_t)(64 * j32 + lane) * D_) + PFD * 64;
    if (lane < 28) {
      st.pf1 = (const char*)((lane < 14 ? w1 : w3) + (size_t)(PFD * 32 + j32) * F_ + xg * 448) + (lane % 14) * 64;
      st.pfs1 = 32u * F_ * 2u;
    } else {
      const int l = (lane - 28) & 7;
      st.pf1 = (const char*)((l < 4 ? bre : bim) + (size_t)(128 * xg + 4 * j32 + (l & 3)) * D_) + PFD * 64;
      st.pfs1 = 64u;
    }
    st.wsdbg = ws + (210u << 20);
    st.c0 = (const char*)cre + (size_t)bid * 8192;
    st.c1 = (const char*)cim + (size_t)bid * 8192;
    st.c2 = (const char*)w2 + (size_t)bid * 28672;
  }

  f32x4 acc[4][9];
#pragma unroll
  for (int m = 0; m < 4; ++m)
#pragma unroll
    for (int i = 0; i < 9; ++i) acc[m][i] = (f32x4){0, 0, 0, 0};
  if (wid < 4)       gemm1_body<0>(lds, st, wid, wm, lane, acc);
  else if (wid < 6)  gemm1_body<1>(lds, st, wid, wm, lane, acc);
  else if (wid == 6) gemm1_body<2>(lds, st, wid, wm, lane, acc);
  else               gemm1_body<3>(lds, st, wid, wm, lane, acc);

  STAMP(ws, 1);
  const int fr = lane & 15, fq = lane >> 4;
  LAS float* U = (LAS float*)lds;
#pragma unroll
  for (int m = 0; m < 4; ++m) {
    const int t = wm * 64 + m * 16 + fr;
    LAS float* u = U + t * 68 + (t >> 4) * 4 + 4 * fq;
    if (wn == 0) {
      *(LAS f32x4*)(u) = acc[m][8];
    } else {
      *(LAS f32x4*)(u + 16) = acc[m][3];
      *(LAS f32x4*)(u + 32) = acc[m][7];
      *(LAS f32x4*)(u + 48) = acc[m][8];
    }
  }
  const int p = wid * 4 + fq, seg = fr;
  asm volatile("s_waitcnt lgkmcnt(0)" ::: "memory");
  __builtin_amdgcn_s_barrier();
  asm volatile("" ::: "memory");
  const f32x4 tb = ((LAS f32x4*)(lds + G1_RING + 2048))[p];
  LAS float* gv = (LAS float*)(lds + G1_RING + 1024);
  const float lr = tb[0], li = tb[1];
  float sr[16], si[16];
#pragma unroll
  for (int i = 0; i < 16; ++i) {
    sr[i] = U[(seg * 16 + i) * 68 + seg * 4 + p];
    si[i] = U[(seg * 16 + i) * 68 + seg * 4 + 32 + p];
  }
#pragma unroll
  for (int i = 1; i < 16; ++i) {
    const float a = sr[i - 1], b = si[i - 1];
    sr[i] += lr * a - li * b;
    si[i] += lr * b + li * a;
  }
  float pr_ = lr, pi_ = li;
#pragma unroll
  for (int i = 0; i < 4; ++i) { const float a = pr_ * pr_ - pi_ * pi_, b = 2.f * pr_ * pi_; pr_ = a; pi_ = b; }
  float tr_ = sr[15], ti_ = si[15];
  float lsr = 1.f, lsi = 0.f;
#pragma unroll
  for (int d = 1; d < 16; d <<= 1) {
    const float ar = __shfl_up(tr_, d, 16), ai = __shfl_up(ti_, d, 16);
    if (seg >= d) { tr_ += pr_ * ar - pi_ * ai; ti_ += pr_ * ai + pi_ * ar; }
    if (seg & d) { const float a = lsr * pr_ - lsi * pi_, b = lsr * pi_ + lsi * pr_; lsr = a; lsi = b; }
    const float a = pr_ * pr_ - pi_ * pi_, b = 2.f * pr_ * pi_; pr_ = a; pi_ = b;
  }
  GAS u64* gran = (GAS u64*)(ws + WS_GRAN) + (size_t)cb * 8 * 64;
  if (seg == 15 && rb < 7) {
    __hip_atomic_store(gran + rb * 64 + p, (1ull << 32) | (u64)__float_as_uint(tr_), RLX_AGENT);
    __hip_atomic_store(gran + rb * 64 + 32 + p, (1ull << 32) | (u64)__float_as_uint(ti_), RLX_AGENT);
  }
  float cr = __shfl_up(tr_, 1, 16), ci = __shfl_up(ti_, 1, 16);
  if (seg == 0) { cr = 0.f; ci = 0.f; }
  STAMP(ws, 2);
  u16* A2 = (u16*)(ws + WS_A2);
  {
    LAS char* HT = lds + 73728;
    const int ng = wn == 0 ? 4 : 3;
#pragma unroll
    for (int m = 0; m < 4; ++m) {
      const int tl = wm * 64 + m * 16 + fr;
      const float omg = 1.0f - gv[tl];
#pragma unroll
      for (int i = 0; i < 4; ++i) {
        if (i < ng) {
          const f32x4 a = acc[m][i];
          const f32x4 au = a * acc[m][4 + i] * omg;
          const f32x4 z = a * -1.44269504089f;
          f32x4 e;
#pragma unroll
          for (int j = 0; j < 4; ++j) e[j] = __builtin_amdgcn_exp2f(z[j]);
          e = e + 1.0f;
          f32x4 r;
#pragma unroll
          for (int j = 0; j < 4; ++j) r[j] = __builtin_amdgcn_rcpf(e[j]);
          const f32x4 h = au * r;
          *(LAS u32x2*)(HT + tl * 240 + (wn * 4 + i) * 32 + fq * 8) = (u32x2){cvt_pk_bf16(h[0], h[1]), cvt_pk_bf16(h[2], h[3])};
        }
      }
    }
    asm volatile("s_waitcnt lgkmcnt(0)" ::: "memory");
    __builtin_amdgcn_s_barrier();
    asm volatile("" ::: "memory");
#pragma unroll
    for (int i = 0; i < 7; ++i) {
      const int idx = tid + 512 * i, t = idx / 14, c = idx % 14, nb = c >> 1;
      const int blk = nb < 4 ? (l0 + c4) * 4 + nb : (c4 < 3 ? (l0 + 4 + c4) * 4 + (nb - 4) : (l0 + nb) * 4 + 3);
      const u32x4 v = *(LAS u32x4*)(HT + t * 240 + c * 16);
      A2_STORE(A2 + (size_t)(rb * 256 + t) * KT_ + 2 * P_ + blk * 16 + (c & 1) * 8, v);
    }
  }
  STAMP(ws, 3);
  float hr = 0.f, hi = 0.f;
  if (seg < rb) {
    u64 v0, v1; unsigned spins = 0;
    for (;;) {
      v0 = __hip_atomic_load(gran + seg * 64 + p, RLX_AGENT);
      v1 = __hip_atomic_load(gran + seg * 64 + 32 + p, RLX_AGENT);
      if ((v0 >> 32) == 1ull && (v1 >> 32) == 1ull) break;
      if (++spins > (1u << 24)) break;
      __builtin_amdgcn_s_sleep(1);
    }
    hr = __uint_as_float((unsigned)v0); hi = __uint_as_float((unsigned)v1);
    for (int e = rb - 1 - seg; e > 0; --e) { const float a = pr_ * hr - pi_ * hi, b = pr_ * hi + pi_ * hr; hr = a; hi = b; }
  }
#pragma unroll
  for (int d = 1; d < 8; d <<= 1) { hr += __shfl_xor(hr, d, 16); hi += __shfl_xor(hi, d, 16); }
  hr = __shfl(hr, 0, 16); hi = __shfl(hi, 0, 16);
  STAMP(ws, 4);
  cr += lsr * hr - lsi * hi;
  ci += lsr * hi + lsi * hr;
  LAS char* HS = lds;
  const float cfr = tb[2], cfi = tb[3];
#pragma unroll
  for (int i = 0; i < 16; ++i) {
    const float nr = lr * cr - li * ci, ni = lr * ci + li * cr;
    cr = nr; ci = ni;
    const float a = sr[i] + cr, b = si[i] + ci;
    const float g2 = 2.0f * gv[seg * 16 + i];
    const float orr = (cfr * a - cfi * b) * g2, oi = -(cfr * b + cfi * a) * g2;
    const unsigned pk = cvt_pk_bf16(orr, oi);
    LAS u16* hp = (LAS u16*)(HS + (seg * 16 + i) * 128 + seg * 16) + p;
    hp[0] = (u16)(pk & 0xffffu);
    hp[32] = (u16)(pk >> 16);
  }
  asm volatile("s_waitcnt lgkmcnt(0)" ::: "memory");
  __builtin_amdgcn_s_barrier();
  asm volatile("" ::: "memory");
#pragma unroll
  for (int i = 0; i < 4; ++i) {
    const int idx = tid + 512 * i, t = idx >> 3, c = idx & 7;
    const u32x4 v = *(LAS u32x4*)(HS + idx * 16 + (t >> 4) * 16);
    A2_STORE(A2 + (size_t)(rb * 256 + t) * KT_ + (c >> 2) * P_ + cb * 32 + (c & 3) * 8, v);
  }
  STAMP(ws, 5);
}

constexpr int G2_STAGE = 32768, G2_NBUF = 4, G2_LDS = G2_STAGE * G2_NBUF;
constexpr int G2_NKT = 44;

__global__ __launch_bounds__(512, 2) void k_gemm2(const u16* __restrict__ x, const u16* __restrict__ cre,
                                                  const u16* __restrict__ cim, const u16* __restrict__ w2,
                                                  const u16* __restrict__ dskip, char* __restrict__ ws,
                                                  float* __restrict__ out) {
  extern __shared__ __attribute__((aligned(16))) char shm[];
  LAS char* lds = (LAS char*)shm;
  const int tid = threadIdx.x, lane = tid & 63;
  const int wid = __builtin_amdgcn_readfirstlane(tid >> 6);
  const int kg = wid >> 2, wm = (wid >> 1) & 1, wn = wid & 1;
  const int bid = blockIdx.x, xcd = bid & 7, idx = bid >> 3;
  const int rt = (xcd >> 1) * 4 + (idx & 3), ct = (xcd & 1) * 4 + ((idx >> 2) & 3), hs = idx >> 4;
  const int fr = lane & 15, fq = lane >> 4;
  const u16* A2 = (const u16*)(ws + WS_A2);
  STAMP(ws, 8);

  const int arow = 8 * wid + (lane >> 3);
  const unsigned cs8 = (unsigned)(((lane & 7) ^ ((arow >> 1) & 7)) * 8);
  const unsigned voA = (unsigned)(arow * KT_ + cs8) * 2u;
  const unsigned voC = (unsigned)(arow * P_ + cs8) * 2u;
  const int wk = 8 * wid + (lane >> 3);
  const unsigned voW = (unsigned)(wk * D_ + ((((lane & 7) >> 1) ^ (((wk >> 1) & 1) | (((wk >> 3) & 1) << 1))) * 16) + (lane & 1) * 8) * 2u;
  const int rt_ = rt, ct_ = ct;
  const rsrc_t ra = mk_rsrc(A2 + (size_t)rt_ * 128 * KT_, 128u * KT_ * 2u);
  const rsrc_t rcr = mk_rsrc(cre + (size_t)ct_ * 128 * P_, 128u * P_ * 2u);
  const rsrc_t rci = mk_rsrc(cim + (size_t)ct_ * 128 * P_, 128u * P_ * 2u);
  const rsrc_t rw = mk_rsrc(w2 + ct_ * 128, (unsigned)(F_ * D_ - ct_ * 128) * 2u);
  const int kt0 = hs * G2_NKT;

  auto piece = [&](int i, int buf, int ktg) {
    LAS char* d = lds + buf * G2_STAGE + wid * 1024;
    if (i == 0) dma16(ra, d, voA, ktg * 128);
    if (i == 1) dma16(ra, d + 8192, voA, ktg * 128 + 64 * KT_ * 2);
    if (i >= 2) {
      if (ktg < 32) {
        const rsrc_t rc = ktg < 16 ? rcr : rci;
        dma16(rc, d + 16384 + (i - 2) * 8192, voC, (ktg & 15) * 128 + (i - 2) * (64 * P_ * 2));
      } else {
        dma16(rw, d + 16384 + (i - 2) * 8192, voW, (ktg - 32) * (64 * D_ * 2) + (i - 2) * 128);
      }
    }
  };
  const int a_off = (wm * 64 + fr) * 128 + (((4 * kg + fq) ^ (fr >> 1)) << 4);
  const int c_off = 16384 + (wn * 64 + fr) * 128 + (((4 * kg + fq) ^ (fr >> 1)) << 4);
  const int q = (lane & 15) >> 2, p = lane & 3;
  const int w_base = 16384 + wn * 8192 + (32 * kg + 8 * fq + q) * 128 + ((((q >> 1) & 1) | ((fq & 1) << 1)) << 5) + p * 8;

  f32x4 acc[4][4];
#pragma unroll
  for (int m = 0; m < 4; ++m)
#pragma unroll
    for (int n = 0; n < 4; ++n) acc[m][n] = (f32x4){0, 0, 0, 0};
  bf16x8 fa[2][4], fb[2][4];

  auto step = [&](auto CUR, auto RT, auto VM, auto DM, int j) {
    constexpr int c = decltype(CUR)::value, rtp = decltype(RT)::value, vm = decltype(VM)::value;
    constexpr bool dm = decltype(DM)::value;
    if (vm >= 0) asm volatile("s_waitcnt vmcnt(%0) lgkmcnt(0)" :: "n"(vm < 0 ? 0 : vm) : "memory");
    else asm volatile("s_waitcnt lgkmcnt(0)" ::: "memory");
    BAR();
    LAS char* base = lds + ((j + 1) & 3) * G2_STAGE;
#pragma unroll
    for (int m = 0; m < 4; ++m) {
      if (rtp < 2) {
        fa[c ^ 1][m] = lds_b128(base + a_off + m * 2048);
        if (rtp == 0) fb[c ^ 1][m] = lds_b128(base + c_off + m * 2048);
        else fb[c ^ 1][m] = lds_tr2(base + (w_base ^ (m << 5)), base + (w_base ^ (m << 5)) + 512);
      }
#pragma unroll
      for (int n = 0; n < 4; ++n) MFMA_IP(acc[m][n], fb[c][n], fa[c][m]);
      if (dm) piece(m, j & 3, kt0 + j + 4);
      SCHED();
    }
  };
  using I0 = std::integral_constant<int, 0>; using I1 = std::integral_constant<int, 1>; using I2 = std::integral_constant<int, 2>;
  using I4 = std::integral_constant<int, 4>; using I8 = std::integral_constant<int, 8>; using IM = std::integral_constant<int, -1>;
  using BT = std::true_type; using BF = std::false_type;

#pragma unroll
  for (int t = 0; t < 4; ++t) {
#pragma unroll
    for (int i = 0; i < 4; ++i) piece(i, t, kt0 + t);
  }
  asm volatile("s_waitcnt vmcnt(12)" ::: "memory");
  BAR();
  STAMP(ws, 9);
  int j = 0;
  if (hs == 0) {
#pragma unroll
    for (int m = 0; m < 4; ++m) { fa[0][m] = lds_b128(lds + a_off + m * 2048); fb[0][m] = lds_b128(lds + c_off + m * 2048); }
#pragma unroll 1
    for (; j < 30; j += 2) { step(I0{}, I0{}, I8{}, BT{}, j); step(I1{}, I0{}, I8{}, BT{}, j + 1); }
    step(I0{}, I0{}, I8{}, BT{}, 30); step(I1{}, I1{}, I8{}, BT{}, 31);
    j = 32;
  } else {
#pragma unroll
    for (int m = 0; m < 4; ++m) {
      fa[0][m] = lds_b128(lds + a_off + m * 2048);
      fb[0][m] = lds_tr2(lds + (w_base ^ (m << 5)), lds + (w_base ^ (m << 5)) + 512);
    }
  }
#pragma unroll 1
  for (; j < 40; j += 2) { step(I0{}, I1{}, I8{}, BT{}, j); step(I1{}, I1{}, I8{}, BT{}, j + 1); }
  step(I0{}, I1{}, I8{}, BF{}, 40); step(I1{}, I1{}, I4{}, BF{}, 41);
  step(I0{}, I1{}, I0{}, BF{}, 42); step(I1{}, I2{}, IM{}, BF{}, 43);
  asm volatile("s_waitcnt vmcnt(0) lgkmcnt(0)\n\ts_nop 7\n\ts_nop 7" ::: "memory");
  BAR();
  asm volatile("" ::: "memory");
  STAMP(ws, 10);
  {
    LAS f32x4* X = (LAS f32x4*)lds;
#pragma unroll
    for (int mm = 0; mm < 2; ++mm)
#pragma unroll
      for (int n = 0; n < 4; ++n) {
        const f32x4 v = kg ? acc[mm][n] : acc[2 + mm][n];
        X[(wid * 8 + mm * 4 + n) * 64 + lane] = v;
      }
    asm volatile("s_waitcnt lgkmcnt(0)" ::: "memory");
    __builtin_amdgcn_s_barrier();
    asm volatile("" ::: "memory");
  }
  f32x4 r[2][4];
  {
    LAS f32x4* X = (LAS f32x4*)lds;
    const int pw = wid ^ 4;
#pragma unroll
    for (int mm = 0; mm < 2; ++mm)
#pragma unroll
      for (int n = 0; n < 4; ++n) {
        const f32x4 own = kg ? acc[2 + mm][n] : acc[mm][n];
        r[mm][n] = own + X[(pw * 8 + mm * 4 + n) * 64 + lane];
      }
  }
  const int tile = rt * 8 + ct;
  char* slab_me = ws + WS_SLAB + (size_t)(tile * 2 + hs) * 32768;
  char* slab_pt = ws + WS_SLAB + (size_t)(tile * 2 + (hs ^ 1)) * 32768;
  GAS unsigned* flag_me = (GAS unsigned*)(ws + WS_FLAG) + (tile * 2 + hs) * 64;
  GAS unsigned* flag_pt = (GAS unsigned*)(ws + WS_FLAG) + (tile * 2 + (hs ^ 1)) * 64;
  const int sw = kg * 2 + wn;
  if (wm != hs) {
    __amdgpu_buffer_rsrc_t rs = __builtin_amdgcn_make_buffer_rsrc(slab_me, 0, 32768, 0x00020000);
#pragma unroll
    for (int mm = 0; mm < 2; ++mm)
#pragma unroll
      for (int n = 0; n < 4; ++n)
        __builtin_amdgcn_raw_buffer_store_b128(__builtin_bit_cast(u32x4, r[mm][n]), rs,
                                               ((sw * 8 + mm * 4 + n) * 64 + lane) * 16, 0, 16);
    asm volatile("s_waitcnt vmcnt(0)" ::: "memory");
  }
  asm volatile("" ::: "memory");
  __builtin_amdgcn_s_barrier();
  asm volatile("" ::: "memory");
  STAMP(ws, 11);
  if (tid == 0) __hip_atomic_store(flag_me, 1u, RLX_AGENT);
  if (wid == (unsigned)(hs * 2)) {
    unsigned spins = 0;
    while ((unsigned)__builtin_amdgcn_readfirstlane(__hip_atomic_load(flag_pt, RLX_AGENT)) != 1u) {
      if (++spins > (1u << 24)) break;
      __builtin_amdgcn_s_sleep(1);
    }
  }
  asm volatile("" ::: "memory");
  __builtin_amdgcn_s_barrier();
  asm volatile("" ::: "memory");
  STAMP(ws, 12);
  if (wm == hs) {
    __amdgpu_buffer_rsrc_t rs = __builtin_amdgcn_make_buffer_rsrc(slab_pt, 0, 32768, 0x00020000);
    u32x4 pv[2][4];
#pragma unroll
    for (int mm = 0; mm < 2; ++mm)
#pragma unroll
      for (int n = 0; n < 4; ++n)
        pv[mm][n] = __builtin_amdgcn_raw_buffer_load_b128(rs, ((sw * 8 + mm * 4 + n) * 64 + lane) * 16, 0, 16);
    const float* gv = (const float*)(ws + WS_G);
#pragma unroll
    for (int mm = 0; mm < 2; ++mm) {
      const int t = rt * 128 + wm * 64 + (2 * kg + mm) * 16 + fr;
      const float g = gv[t];
#pragma unroll
      for (int n = 0; n < 4; ++n) {
        const int d = ct * 128 + wn * 64 + n * 16 + 4 * fq;
        const u32x2 xv = *(const u32x2*)(x + (size_t)t * D_ + d);
        const u32x2 dv = *(const u32x2*)(dskip + d);
        f32x4 o = r[mm][n] + __builtin_bit_cast(f32x4, pv[mm][n]);
        o[0] += g * rbf(bf2f(xv[0] & 0xffffu) * bf2f(dv[0] & 0xffffu));
        o[1] += g * rbf(bf2f(xv[0] >> 16) * bf2f(dv[0] >> 16));
        o[2] += g * rbf(bf2f(xv[1] & 0xffffu) * bf2f(dv[1] & 0xffffu));
        o[3] += g * rbf(bf2f(xv[1] >> 16) * bf2f(dv[1] >> 16));
        *(f32x4*)(out + (size_t)t * D_ + d) = o;
      }
    }
  }
  STAMP(ws, 13);
}

extern "C" void kernel_launch(void* const* d_in, const int* in_sizes, int n_in, void* d_out, int out_size,
                              void* d_ws, size_t ws_size, hipStream_t stream) {
  const u16* x = (const u16*)d_in[0];
  const u16* lre = (const u16*)d_in[1];
  const u16* lim = (const u16*)d_in[2];
  const u16* bre = (const u16*)d_in[3];
  const u16* bim = (const u16*)d_in[4];
  const u16* cre = (const u16*)d_in[5];
  const u16* cim = (const u16*)d_in[6];
  const u16* ldt = (const u16*)d_in[7];
  const u16* dsk = (const u16*)d_in[8];
  const u16* w1 = (const u16*)d_in[10];
  const u16* w3 = (const u16*)d_in[11];
  const u16* w2 = (const u16*)d_in[12];
  const u16* gw = (const u16*)d_in[13];
  char* ws = (char*)d_ws;
  hipFuncSetAttribute((const void*)k_gemm1, hipFuncAttributeMaxDynamicSharedMemorySize, G1_LDS);
  hipFuncSetAttribute((const void*)k_gemm2, hipFuncAttributeMaxDynamicSharedMemorySize, G2_LDS);
  k_prep<<<256, 512, 0, stream>>>(x, gw, lre, lim, ldt, ws, w1, w3, bre, bim, cre, cim, w2);
  k_gemm1<<<256, 512, G1_LDS, stream>>>(x, w1, w3, bre, bim, ws, cre, cim, w2);
  k_gemm2<<<256, 512, G2_LDS, stream>>>(x, cre, cim, w2, dsk, ws, (float*)d_out);
}
```

```cpp
#include <hip/hip_runtime.h>
#include <hip/hip_bf16.h>
#include <stdint.h>
#include <type_traits>

typedef unsigned short u16;
typedef unsigned long long u64;
typedef short bf16x8 __attribute__((ext_vector_type(8)));
typedef short s16x4 __attribute__((ext_vector_type(4)));
typedef float f32x4 __attribute__((ext_vector_type(4)));
typedef float f32x2 __attribute__((ext_vector_type(2)));
typedef unsigned u32x4 __attribute__((ext_vector_type(4)));
typedef unsigned u32x2 __attribute__((ext_vector_type(2)));
#define LAS __attribute__((address_space(3)))
#define GAS __attribute__((address_space(1)))
#define RLX_AGENT __ATOMIC_RELAXED, __HIP_MEMORY_SCOPE_AGENT

#define PFD 6
constexpr int S_ = 2048, D_ = 1024, P_ = 1024, F_ = 3584, KT_ = 2 * P_ + F_;

constexpr size_t WS_GRAN = 0;
constexpr size_t WS_FLAG = 131072;
constexpr size_t WS_G    = WS_FLAG + 65536;
constexpr size_t WS_TAB  = WS_G + 8192;
constexpr size_t WS_A2   = WS_TAB + 16384;
constexpr size_t WS_SLAB = WS_A2 + (size_t)S_ * KT_ * 2;

#define STAMP(ws, k) do {} while (0)
__device__ __forceinline__ float bf2f(unsigned v16) { return __uint_as_float(v16 << 16); }
__device__ __forceinline__ unsigned cvt_pk_bf16(float lo, float hi) {
  unsigned r; asm("v_cvt_pk_bf16_f32 %0, %1, %2" : "=v"(r) : "v"(lo), "v"(hi)); return r;
}
__device__ __forceinline__ float rbf(float f) { return __uint_as_float(cvt_pk_bf16(f, f) << 16); }

__global__ __launch_bounds__(512) void k_prep(const u16* __restrict__ x, const u16* __restrict__ gw,
                                              const u16* __restrict__ lre, const u16* __restrict__ lim,
                                              const u16* __restrict__ ldt, char* __restrict__ ws,
                                              const u16* __restrict__ w1, const u16* __restrict__ w3,
                                              const u16* __restrict__ bre, const u16* __restrict__ bim,
                                              const u16* __restrict__ cre, const u16* __restrict__ cim,
                                              const u16* __restrict__ w2) {
  const int tid = threadIdx.x, wid = tid >> 6, lane = tid & 63, b = blockIdx.x;
  const int pch = b * 4 + (tid & 3);
  const unsigned in_lr = lre[pch], in_li = lim[pch], in_dt = ldt[pch];
  u32x4 wacc = {0u, 0u, 0u, 0u};
  {
    const int xg = b & 7, j32 = b >> 3;
    constexpr int CPR = PFD * 4;
    constexpr int NX = (64 * CPR + 511) / 512, NW = (PFD * 2 * 56 + 511) / 512;
    u32x4 v[NX + NW + 1];
#pragma unroll
    for (int i = 0; i < NX; ++i) {
      int c = tid + i * 512; c = c < 64 * CPR ? c : 0;
      v[i] = *(const u32x4*)(x + (size_t)(64 * j32 + c / CPR) * D_ + (c % CPR) * 8);
    }
#pragma unroll
    for (int i = 0; i < NW; ++i) {
      int c = tid + i * 512; c = c < PFD * 2 * 56 ? c : 0;
      const int mat = c / (PFD * 56), r = c % (PFD * 56);
      v[NX + i] = *(const u32x4*)((mat ? w3 : w1) + (size_t)(j32 * PFD + r / 56) * F_ + xg * 448 + (r % 56) * 8);
    }
    {
      const int c = tid < 8 * CPR ? tid : 0, r = c / CPR;
      v[NX + NW] = *(const u32x4*)((r < 4 ? bre : bim) + (size_t)(128 * xg + 4 * j32 + (r & 3)) * D_ + (c % CPR) * 8);
    }
#pragma unroll
    for (int i = 0; i < NX + NW + 1; ++i) wacc ^= v[i];
  }
  if (tid < 64) ((u64*)(ws + WS_GRAN))[b * 64 + tid] = 0ull;
  if (tid == 64) ((unsigned*)(ws + WS_FLAG))[b * 64] = 0u;
  const int row = b * 8 + wid;
  const u32x4* xp = (const u32x4*)(x + (size_t)row * D_);
  const u32x4* gp = (const u32x4*)gw;
  float s = 0.f;
#pragma unroll
  for (int i = 0; i < 2; ++i) {
    u32x4 a = xp[lane + 64 * i], c = gp[lane + 64 * i];
#pragma unroll
    for (int j = 0; j < 4; ++j) {
      s += rbf(bf2f(a[j] & 0xffffu) * bf2f(c[j] & 0xffffu));
      s += rbf(bf2f(a[j] >> 16) * bf2f(c[j] >> 16));
    }
  }
#pragma unroll
  for (int o = 32; o >= 1; o >>= 1) s += __shfl_xor(s, o);
  if (lane == 0) {
    s = rbf(s);
    float g = rbf(1.0f / (1.0f + expf(-s)));
    ((float*)(ws + WS_G))[row] = g;
  }
  asm volatile("" :: "v"(wacc));
  if (tid >= 448 && tid < 452) {
    const int p = pch;
    const float lr = bf2f(in_lr), li = bf2f(in_li);
    const float dt = rbf(expf(bf2f(in_dt)));
    const float zr = dt * lr, zi = dt * li;
    const float e = expf(zr);
    float sn, cs; sincosf(zi, &sn, &cs);
    const float br = e * cs, bi = e * sn;
    const float nr = br - 1.0f, ni = bi;
    const float den = lr * lr + li * li;
    const float cr = (nr * lr + ni * li) / den, ci = (ni * lr - nr * li) / den;
    ((f32x4*)(ws + WS_TAB))[p] = (f32x4){br, bi, cr, ci};
  }
}

constexpr int G1_SLOT = 36864;
constexpr int G1_WG = 16384, G1_WU = 24576, G1_BS = 32768;
constexpr int G1_RING = 4 * G1_SLOT;
constexpr int G1_LDS = G1_RING + 8192;

typedef __amdgpu_buffer_rsrc_t rsrc_t;
__device__ __forceinline__ rsrc_t mk_rsrc(const void* p, unsigned bytes) {
  return __builtin_amdgcn_make_buffer_rsrc((void*)p, 0, bytes, 0x00020000);
}
__device__ __forceinline__ void dma16(rsrc_t rs, LAS char* dst, unsigned voff, unsigned soff) {
  asm volatile("s_mov_b32 m0, %0\n\ts_nop 0\n\tbuffer_load_dwordx4 %1, %2, %3 offen lds"
               :: "s"((unsigned)(size_t)dst), "v"(voff), "s"(rs), "s"(soff) : "memory");
}
__device__ __forceinline__ void touch4(rsrc_t rs, LAS char* dst, unsigned voff, unsigned soff) {
  soff = __builtin_amdgcn_readfirstlane(soff);
  asm volatile("" : "+s"(soff));
  asm volatile("s_mov_b32 m0, %0\n\ts_nop 0\n\tbuffer_load_dword %1, %2, %3 offen lds"
               :: "s"((unsigned)(size_t)dst), "v"(voff), "s"(rs), "s"(soff) : "memory");
}
__device__ __forceinline__ void touchg(const char* p, LAS char* dst) {
  asm volatile("s_mov_b32 m0, %0\n\ts_nop 0\n\tglobal_load_lds_dword %1, off" :: "s"((unsigned)(size_t)dst), "v"(p) : "memory");
}
__device__ __forceinline__ bf16x8 lds_b128(LAS char* p) { return *(LAS bf16x8*)p; }
__device__ __forceinline__ bf16x8 lds_tr2(LAS char* p0, LAS char* p1) {
  s16x4 a = __builtin_amdgcn_ds_read_tr16_b64_v4i16((LAS s16x4*)p0);
  s16x4 b = __builtin_amdgcn_ds_read_tr16_b64_v4i16((LAS s16x4*)p1);
  return __builtin_shufflevector(a, b, 0, 1, 2, 3, 4, 5, 6, 7);
}
#define MFMA(a, b, c) __builtin_amdgcn_mfma_f32_16x16x32_bf16((a), (b), (c), 0, 0, 0)
#define MFMA_IP(c, a, b) asm volatile("v_mfma_f32_16x16x32_bf16 %0, %1, %2, %0" : "+v"(c) : "v"(a), "v"(b))

#define A2_STORE(p, v) (*(u32x4*)(p) = (v))
#define SCHED() __builtin_amdgcn_sched_barrier(0)
#define BAR() do { SCHED(); __builtin_amdgcn_s_barrier(); SCHED(); } while (0)

struct G1Stage {
  rsrc_t rx, rw, r4;
  unsigned voA, voW, voW2;
  unsigned vo4, vo5;
  const char* pf0; const char* pf1; unsigned pfs1;
  char* wsdbg;
  const char *c0, *c1, *c2;
};

template <int ROLE>
__device__ __forceinline__ void gemm1_body(LAS char* lds, G1Stage& st, int wid, int wm, int lane, f32x4 (&acc)[4][9]) {
  constexpr int WN = ROLE == 0 ? 0 : 1;
  constexpr int NG = WN == 0 ? 4 : 3;
  constexpr int NB0 = WN == 0 ? 0 : 4;
  constexpr int S0 = WN == 0 ? 0 : 1;
  constexpr int NDMA = ROLE == 2 ? 6 : 5;
  const int fr = lane & 15, fq = lane >> 4;
  const int frag = fr * 64 + ((fq ^ ((fr >> 3) << 1)) << 4);
  const int a_off = wm * 4096 + frag;
  const int s_off = G1_BS + S0 * 1024 + frag;
  const int q = (lane & 15) >> 2, p = lane & 3;
  const int w_base = G1_WG + WN * 4096 + (8 * fq + q) * 128 + ((((q >> 1) & 1) | ((fq & 1) << 1)) << 5) + p * 8;

  auto piece = [&](int i, int slot, int j) {
    LAS char* d = lds + slot * G1_SLOT;
    if (ROLE < 3) {
      if (i == 0) dma16(st.rx, d + wid * 1024, st.voA, j * 64 + wid * 32768);
      if (i == 1) dma16(st.rx, d + wid * 1024 + 7168, st.voA, j * 64 + wid * 32768 + 7 * 32768);
      if (i == 2) dma16(st.rw, d + G1_WG + (wid >> 2) * 8192 + (wid & 3) * 1024, st.voW, j * (32 * F_ * 2));
      if (i == 3) dma16(st.rw, d + G1_WG + (wid >> 2) * 8192 + (wid & 3) * 1024 + 4096, st.voW2, j * (32 * F_ * 2));
      if (i == 4) {
        if (ROLE == 0) dma16(st.r4, d + G1_BS + wid * 1024, st.voA, j * 64);
        if (ROLE == 1) dma16(st.rx, d + (10 + wid) * 1024, st.voA, j * 64 + (10 + wid) * 32768);
        if (ROLE == 2) dma16(st.rw, d + G1_WG + 8192 + 3 * 1024, st.vo4, j * (32 * F_ * 2));
      }
      if (i == 5 && ROLE == 2) dma16(st.rw, d + G1_WG + 8192 + 3 * 1024 + 4096, st.vo5, j * (32 * F_ * 2));
    }
  };
  auto stage = [&](int slot, int j) {
#pragma unroll
    for (int i = 0; i < 6; ++i) piece(i, slot, j);
  };
  auto touch = [&](int j) {
    if (ROLE == 3) {
      touchg(st.pf0, lds + G1_RING); st.pf0 += 64;
      touchg(st.pf1, lds + G1_RING + 256); st.pf1 += st.pfs1;
    }
  };
  auto step = [&](int slot, int j) {
    LAS char* base = lds + slot * G1_SLOT;
    const bool pre = j + 3 < 32;
    const int ns = (slot + 3) & 3;
    bf16x8 xa[4], fb[9];
    if (pre) piece(0, ns, j + 3);
#pragma unroll
    for (int m = 0; m < 4; ++m) xa[m] = lds_b128(base + a_off + m * 1024);
#pragma unroll
    for (int i = 0; i < NG; ++i) {
      fb[i] = lds_tr2(base + (w_base ^ (i << 5)), base + (w_base ^ (i << 5)) + 512);
      fb[4 + i] = lds_tr2(base + (w_base ^ (i << 5)) + (G1_WU - G1_WG), base + (w_base ^ (i << 5)) + 512 + (G1_WU - G1_WG));
    }
    if (WN == 0) fb[8] = lds_b128(base + s_off);
    else { fb[3] = lds_b128(base + s_off); fb[7] = lds_b128(base + s_off + 1024); fb[8] = lds_b128(base + s_off + 2048); }
    if (ROLE == 3) {
      if (j + PFD < 32) touch(j + PFD);
      if (j >= 16 && j < 27) {
        const int jj = j - 16;
        const char* pb = jj < 2 ? st.c0 + jj * 4096 : (jj < 4 ? st.c1 + (jj - 2) * 4096 : st.c2 + (jj - 4) * 4096);
        touchg(pb + lane * 64, lds + G1_RING + 512);
      }
      asm volatile("s_waitcnt lgkmcnt(0)" ::: "memory");
    } else if (pre) {
      piece(1, ns, j + 3);
      asm volatile("s_waitcnt vmcnt(%0) lgkmcnt(0)" :: "n"(NDMA + 2) : "memory");
    } else if (j + 3 == 32) {
      asm volatile("s_waitcnt vmcnt(%0) lgkmcnt(0)" :: "n"(NDMA) : "memory");
    } else {
      asm volatile("s_waitcnt vmcnt(0) lgkmcnt(0)" ::: "memory");
    }
    BAR();
    __builtin_amdgcn_s_setprio(1);
#pragma unroll
    for (int i = 0; i < 9; ++i) {
#pragma unroll
      for (int m = 0; m < 4; ++m) MFMA_IP(acc[m][i], fb[i], xa[m]);
      if (pre) {
        if (i == 1) piece(2, ns, j + 3);
        if (i == 3) piece(3, ns, j + 3);
        if (i == 5) piece(4, ns, j + 3);
        if (i == 7) piece(5, ns, j + 3);
      }
    }
    __builtin_amdgcn_s_setprio(0);
    BAR();
  };

  stage(0, 0); stage(1, 1); stage(2, 2);
  if (ROLE == 3) {
    dma16(st.rx, lds + G1_RING + 1024, (unsigned)lane * 16u, 0u);
    dma16(st.r4, lds + G1_RING + 2048, (unsigned)lane * 16u, 0u);
  } else {
    asm volatile("s_waitcnt vmcnt(%0)" :: "n"(2 * NDMA) : "memory");
  }
  BAR();
  if (WN == 1) BAR();
#pragma unroll 1
  for (int j = 0; j < 28; j += 4) {
    step(0, j); step(1, j + 1); step(2, j + 2); step(3, j + 3);
  }
  step(0, 28); step(1, 29); step(2, 30); step(3, 31);
  if (WN == 0) BAR();
  asm volatile("s_waitcnt vmcnt(0)\n\ts_nop 7\n\ts_nop 7" ::: "memory");
  SCHED();
}

__global__ __launch_bounds__(512, 2) void k_gemm1(const u16* __restrict__ x, const u16* __restrict__ w1,
                                                  const u16* __restrict__ w3, const u16* __restrict__ bre,
                                                  const u16* __restrict__ bim, char* __restrict__ ws,
                                                  const u16* __restrict__ cre, const u16* __restrict__ cim,
                                                  const u16* __restrict__ w2) {
  extern __shared__ __attribute__((aligned(16))) char shm[];
  LAS char* lds = (LAS char*)shm;
  const int tid = threadIdx.x, lane = tid & 63;
  const int wid = __builtin_amdgcn_readfirstlane(tid >> 6);
  const int wm = wid & 3, wn = wid >> 2;
  const int bid = blockIdx.x;
  const int cb = (bid & 7) * 4 + ((bid >> 3) & 3), rb = bid >> 5;
  STAMP(ws, 0);

  G1Stage st;
  const int pr = lane >> 2;
  st.voA = (unsigned)(pr * D_ + (((lane & 3) ^ ((pr >> 3) << 1)) * 8)) * 2u;
  const int c4 = (bid >> 3) & 3, l0 = (bid & 7) * 7;
  {
    const int r = lane >> 3, pos = lane & 7;
    auto woff = [&](int kb, int half) -> unsigned {
      const int k = 8 * kb + r, f = ((k >> 1) & 1) | (((k >> 3) & 1) << 1), b = (pos >> 1) ^ f;
      int blk;
      if (half == 0) blk = (l0 + c4) * 4 + b;
      else blk = c4 < 3 ? (l0 + 4 + c4) * 4 + b : (l0 + 4 + b) * 4 + 3;
      return (half == 1 && b == 3) ? 0x80000000u : (unsigned)(k * F_ + blk * 16 + (pos & 1) * 8) * 2u;
    };
    st.voW = woff(wid & 3, 0); st.voW2 = woff(wid & 3, 1);
    st.vo4 = woff(3, 0); st.vo5 = woff(3, 1);
  }
  st.rx = mk_rsrc(x + (size_t)rb * 256 * D_, 256u * D_ * 2u);
  st.rw = mk_rsrc(wn ? w3 : w1, (unsigned)(D_ * F_) * 2u);
  st.r4 = mk_rsrc((wid < 2 ? bre : bim) + (size_t)(cb * 32 + (wid & 1) * 16) * D_, 16u * D_ * 2u);
  if (wid == 7) {
    st.rx = mk_rsrc(ws + WS_G + rb * 1024, 1024u);
    st.r4 = mk_rsrc(ws + WS_TAB + cb * 512, 512u);
  }
  {
    const int xg = bid & 7, j32 = bid >> 3;
    st.pf0 = (const char*)(x + (size_t)(64 * j32 + lane) * D_) + PFD * 64;
    if (lane < 28) {
      st.pf1 = (const char*)((lane < 14 ? w1 : w3) + (size_t)(PFD * 32 + j32) * F_ + xg * 448) + (lane % 14) * 64;
      st.pfs1 = 32u * F_ * 2u;
    } else {
      const int l = (lane - 28) & 7;
      st.pf1 = (const char*)((l < 4 ? bre : bim) + (size_t)(128 * xg + 4 * j32 + (l & 3)) * D_) + PFD * 64;
      st.pfs1 = 64u;
    }
    st.wsdbg = ws + (210u << 20);
    st.c0 = (const char*)cre + (size_t)bid * 8192;
    st.c1 = (const char*)cim + (size_t)bid * 8192;
    st.c2 = (const char*)w2 + (size_t)bid * 28672;
  }

  f32x4 acc[4][9];
#pragma unroll
  for (int m = 0; m < 4; ++m)
#pragma unroll
    for (int i = 0; i < 9; ++i) acc[m][i] = (f32x4){0, 0, 0, 0};
  if (wid < 4)       gemm1_body<0>(lds, st, wid, wm, lane, acc);
  else if (wid < 6)  gemm1_body<1>(lds, st, wid, wm, lane, acc);
  else if (wid == 6) gemm1_body<2>(lds, st, wid, wm, lane, acc);
  else               gemm1_body<3>(lds, st, wid, wm, lane, acc);

  STAMP(ws, 1);
  const int fr = lane & 15, fq = lane >> 4;
  LAS float* U = (LAS float*)lds;
#pragma unroll
  for (int m = 0; m < 4; ++m) {
    const int t = wm * 64 + m * 16 + fr;
    LAS float* u = U + t * 68 + (t >> 4) * 4 + 4 * fq;
    if (wn == 0) {
      *(LAS f32x4*)(u) = acc[m][8];
    } else {
      *(LAS f32x4*)(u + 16) = acc[m][3];
      *(LAS f32x4*)(u + 32) = acc[m][7];
      *(LAS f32x4*)(u + 48) = acc[m][8];
    }
  }
  const int p = wid * 4 + fq, seg = fr;
  asm volatile("s_waitcnt lgkmcnt(0)" ::: "memory");
  __builtin_amdgcn_s_barrier();
  asm volatile("" ::: "memory");
  const f32x4 tb = ((LAS f32x4*)(lds + G1_RING + 2048))[p];
  LAS float* gv = (LAS float*)(lds + G1_RING + 1024);
  const float lr = tb[0], li = tb[1];
  float sr[16], si[16];
#pragma unroll
  for (int i = 0; i < 16; ++i) {
    sr[i] = U[(seg * 16 + i) * 68 + seg * 4 + p];
    si[i] = U[(seg * 16 + i) * 68 + seg * 4 + 32 + p];
  }
#pragma unroll
  for (int i = 1; i < 16; ++i) {
    const float a = sr[i - 1], b = si[i - 1];
    sr[i] += lr * a - li * b;
    si[i] += lr * b + li * a;
  }
  float pr_ = lr, pi_ = li;
#pragma unroll
  for (int i = 0; i < 4; ++i) { const float a = pr_ * pr_ - pi_ * pi_, b = 2.f * pr_ * pi_; pr_ = a; pi_ = b; }
  float tr_ = sr[15], ti_ = si[15];
  float lsr = 1.f, lsi = 0.f;
#pragma unroll
  for (int d = 1; d < 16; d <<= 1) {
    const float ar = __shfl_up(tr_, d, 16), ai = __shfl_up(ti_, d, 16);
    if (seg >= d) { tr_ += pr_ * ar - pi_ * ai; ti_ += pr_ * ai + pi_ * ar; }
    if (seg & d) { const float a = lsr * pr_ - lsi * pi_, b = lsr * pi_ + lsi * pr_; lsr = a; lsi = b; }
    const float a = pr_ * pr_ - pi_ * pi_, b = 2.f * pr_ * pi_; pr_ = a; pi_ = b;
  }
  GAS u64* gran = (GAS u64*)(ws + WS_GRAN) + (size_t)cb * 8 * 64;
  if (seg == 15 && rb < 7) {
    __hip_atomic_store(gran + rb * 64 + p, (1ull << 32) | (u64)__float_as_uint(tr_), RLX_AGENT);
    __hip_atomic_store(gran + rb * 64 + 32 + p, (1ull << 32) | (u64)__float_as_uint(ti_), RLX_AGENT);
  }
  float cr = __shfl_up(tr_, 1, 16), ci = __shfl_up(ti_, 1, 16);
  if (seg == 0) { cr = 0.f; ci = 0.f; }
  STAMP(ws, 2);
  u16* A2 = (u16*)(ws + WS_A2);
  {
    LAS char* HT = lds + 73728;
    const int ng = wn == 0 ? 4 : 3;
#pragma unroll
    for (int m = 0; m < 4; ++m) {
      const int tl = wm * 64 + m * 16 + fr;
      const float omg = 1.0f - gv[tl];
#pragma unroll
      for (int i = 0; i < 4; ++i) {
        if (i < ng) {
          const f32x4 a = acc[m][i];
          const f32x4 au = a * acc[m][4 + i] * omg;
          const f32x4 z = a * -1.44269504089f;
          f32x4 e;
#pragma unroll
          for (int j = 0; j < 4; ++j) e[j] = __builtin_amdgcn_exp2f(z[j]);
          e = e + 1.0f;
          f32x4 r;
#pragma unroll
          for (int j = 0; j < 4; ++j) r[j] = __builtin_amdgcn_rcpf(e[j]);
          const f32x4 h = au * r;
          *(LAS u32x2*)(HT + tl * 240 + (wn * 4 + i) * 32 + fq * 8) = (u32x2){cvt_pk_bf16(h[0], h[1]), cvt_pk_bf16(h[2], h[3])};
        }
      }
    }
    asm volatile("s_waitcnt lgkmcnt(0)" ::: "memory");
    __builtin_amdgcn_s_barrier();
    asm volatile("" ::: "memory");
#pragma unroll
    for (int i = 0; i < 7; ++i) {
      const int idx = tid + 512 * i, t = idx / 14, c = idx % 14, nb = c >> 1;
      const int blk = nb < 4 ? (l0 + c4) * 4 + nb : (c4 < 3 ? (l0 + 4 + c4) * 4 + (nb - 4) : (l0 + nb) * 4 + 3);
      const u32x4 v = *(LAS u32x4*)(HT + t * 240 + c * 16);
      A2_STORE(A2 + (size_t)(rb * 256 + t) * KT_ + 2 * P_ + blk * 16 + (c & 1) * 8, v);
    }
  }
  STAMP(ws, 3);
  float hr = 0.f, hi = 0.f;
  if (seg < rb) {
    u64 v0, v1; unsigned spins = 0;
    for (;;) {
      v0 = __hip_atomic_load(gran + seg * 64 + p, RLX_AGENT);
      v1 = __hip_atomic_load(gran + seg * 64 + 32 + p, RLX_AGENT);
      if ((v0 >> 32) == 1ull && (v1 >> 32) == 1ull) break;
      if (++spins > (1u << 24)) break;
      __builtin_amdgcn_s_sleep(1);
    }
    hr = __uint_as_float((unsigned)v0); hi = __uint_as_float((unsigned)v1);
    for (int e = rb - 1 - seg; e > 0; --e) { const float a = pr_ * hr - pi_ * hi, b = pr_ * hi + pi_ * hr; hr = a; hi = b; }
  }
#pragma unroll
  for (int d = 1; d < 8; d <<= 1) { hr += __shfl_xor(hr, d, 16); hi += __shfl_xor(hi, d, 16); }
  hr = __shfl(hr, 0, 16); hi = __shfl(hi, 0, 16);
  STAMP(ws, 4);
  cr += lsr * hr - lsi * hi;
  ci += lsr * hi + lsi * hr;
  LAS char* HS = lds;
  const float cfr = tb[2], cfi = tb[3];
#pragma unroll
  for (int i = 0; i < 16; ++i) {
    const float nr = lr * cr - li * ci, ni = lr * ci + li * cr;
    cr = nr; ci = ni;
    const float a = sr[i] + cr, b = si[i] + ci;
    const float g2 = 2.0f * gv[seg * 16 + i];
    const float orr = (cfr * a - cfi * b) * g2, oi = -(cfr * b + cfi * a) * g2;
    const unsigned pk = cvt_pk_bf16(orr, oi);
    LAS u16* hp = (LAS u16*)(HS + (seg * 16 + i) * 128 + seg * 16) + p;
    hp[0] = (u16)(pk & 0xffffu);
    hp[32] = (u16)(pk >> 16);
  }
  asm volatile("s_waitcnt lgkmcnt(0)" ::: "memory");
  __builtin_amdgcn_s_barrier();
  asm volatile("" ::: "memory");
#pragma unroll
  for (int i = 0; i < 4; ++i) {
    const int idx = tid + 512 * i, t = idx >> 3, c = idx & 7;
    const u32x4 v = *(LAS u32x4*)(HS + idx * 16 + (t >> 4) * 16);
    A2_STORE(A2 + (size_t)(rb * 256 + t) * KT_ + (c >> 2) * P_ + cb * 32 + (c & 3) * 8, v);
  }
  STAMP(ws, 5);
}

constexpr int G2_STAGE = 32768, G2_NBUF = 4, G2_LDS = G2_STAGE * G2_NBUF;
constexpr int G2_NKT = 44;

__global__ __launch_bounds__(512, 2) void k_gemm2(const u16* __restrict__ x, const u16* __restrict__ cre,
                                                  const u16* __restrict__ cim, const u16* __restrict__ w2,
                                                  const u16* __restrict__ dskip, char* __restrict__ ws,
                                                  float* __restrict__ out) {
  extern __shared__ __attribute__((aligned(16))) char shm[];
  LAS char* lds = (LAS char*)shm;
  const int tid = threadIdx.x, lane = tid & 63;
  const int wid = __builtin_amdgcn_readfirstlane(tid >> 6);
  const int bid = blockIdx.x, xcd = bid & 7, idx = bid >> 3;
  const int rt = (xcd >> 1) * 4 + (idx & 3), ct = (xcd & 1) * 4 + ((idx >> 2) & 3), hs = idx >> 4;
  const int fr = lane & 15, fq = lane >> 4;
  const int wm = (wid >> 1) & 1, wn = wid & 1;
  const u16* A2 = (const u16*)(ws + WS_A2);
  const int kt0 = hs * G2_NKT;
  STAMP(ws, 8);
  f32x4 acc[4][4];
#pragma unroll
  for (int m = 0; m < 4; ++m)
#pragma unroll
    for (int n = 0; n < 4; ++n) acc[m][n] = (f32x4){0, 0, 0, 0};

  if (wid >= 4) {
    const int l = wid - 4;
    const int arow = 8 * l + (lane >> 3);
    const unsigned cs8 = (unsigned)(((lane & 7) ^ ((arow >> 1) & 7)) * 8);
    const unsigned voA = (unsigned)(arow * KT_ + cs8) * 2u;
    const unsigned voC = (unsigned)(arow * P_ + cs8) * 2u;
    const unsigned voW = (unsigned)(arow * D_ + ((((lane & 7) >> 1) ^ (((arow >> 1) & 1) | (((arow >> 3) & 1) << 1))) * 16) + (lane & 1) * 8) * 2u;
    const int rt_ = rt, ct_ = ct;
    const rsrc_t ra = mk_rsrc(A2 + (size_t)rt_ * 128 * KT_, 128u * KT_ * 2u);
    const rsrc_t rcr = mk_rsrc(cre + (size_t)ct_ * 128 * P_, 128u * P_ * 2u);
    const rsrc_t rci = mk_rsrc(cim + (size_t)ct_ * 128 * P_, 128u * P_ * 2u);
    const rsrc_t rw = mk_rsrc(w2 + ct_ * 128, (unsigned)(F_ * D_ - ct_ * 128) * 2u);
    auto stage = [&](int buf, int ktg) {
      LAS char* d = lds + buf * G2_STAGE + l * 1024;
#pragma unroll
      for (int i = 0; i < 4; ++i) dma16(ra, d + i * 4096, voA, ktg * 128 + i * (32 * KT_ * 2));
      if (ktg < 32) {
        const rsrc_t rc = ktg < 16 ? rcr : rci;
#pragma unroll
        for (int i = 0; i < 4; ++i) dma16(rc, d + 16384 + i * 4096, voC, (ktg & 15) * 128 + i * (32 * P_ * 2));
      } else {
#pragma unroll
        for (int i = 0; i < 4; ++i)
          dma16(rw, d + 16384 + i * 4096, voW, (ktg - 32) * (64 * D_ * 2) + (i & 1) * (32 * D_ * 2) + (i >> 1) * 128);
      }
    };
    stage(0, kt0); stage(1, kt0 + 1); stage(2, kt0 + 2); stage(3, kt0 + 3);
    asm volatile("s_waitcnt vmcnt(24)" ::: "memory");
    BAR();
#pragma unroll 1
    for (int j = 0; j < G2_NKT - 4; ++j) {
      asm volatile("s_waitcnt vmcnt(16)" ::: "memory");
      BAR();
      stage(j & 3, kt0 + j + 4);
    }
    asm volatile("s_waitcnt vmcnt(16)" ::: "memory"); BAR();
    asm volatile("s_waitcnt vmcnt(8)" ::: "memory"); BAR();
    asm volatile("s_waitcnt vmcnt(0)" ::: "memory"); BAR();
  } else {
    const int a_off = (wm * 64 + fr) * 128 + ((fq ^ (fr >> 1)) << 4);
    const int c_off = 16384 + (wn * 64 + fr) * 128 + ((fq ^ (fr >> 1)) << 4);
    const int q = (lane & 15) >> 2, p = lane & 3;
    const int w_base = 16384 + wn * 8192 + (8 * fq + q) * 128 + ((((q >> 1) & 1) | ((fq & 1) << 1)) << 5) + p * 8;
    bf16x8 fa[2][4], fb[2][4];
    auto rd = [&](auto SET, auto TYPE, int slot, int m) {
      constexpr int c = decltype(SET)::value, ty = decltype(TYPE)::value;
      LAS char* base = lds + slot * G2_STAGE;
      fa[c][m] = lds_b128(base + ((a_off + m * 2048) ^ (c * 64)));
      if (ty == 0) fb[c][m] = lds_b128(base + ((c_off + m * 2048) ^ (c * 64)));
      else fb[c][m] = lds_tr2(base + (w_base ^ (m << 5)) + c * 4096, base + (w_base ^ (m << 5)) + c * 4096 + 512);
    };
    using I0 = std::integral_constant<int, 0>; using I1 = std::integral_constant<int, 1>; using I2 = std::integral_constant<int, 2>;
    auto tile = [&](auto TY, auto NT, int j) {
      constexpr int nt = decltype(NT)::value;
#pragma unroll
      for (int m = 0; m < 4; ++m) {
        rd(I1{}, TY, j & 3, m);
#pragma unroll
        for (int n = 0; n < 4; ++n) MFMA_IP(acc[m][n], fb[0][n], fa[0][m]);
        SCHED();
      }
      asm volatile("s_waitcnt lgkmcnt(0)" ::: "memory");
      if (nt < 2) BAR();
#pragma unroll
      for (int m = 0; m < 4; ++m) {
        if (nt == 0) rd(I0{}, I0{}, (j + 1) & 3, m);
        if (nt == 1) rd(I0{}, I1{}, (j + 1) & 3, m);
#pragma unroll
        for (int n = 0; n < 4; ++n) MFMA_IP(acc[m][n], fb[1][n], fa[1][m]);
        SCHED();
      }
    };
    BAR();
    STAMP(ws, 9);
    int j = 0;
    if (hs == 0) {
#pragma unroll
      for (int m = 0; m < 4; ++m) rd(I0{}, I0{}, 0, m);
#pragma unroll 1
      for (; j < 31; ++j) tile(I0{}, I0{}, j);
      tile(I0{}, I1{}, 31);
      j = 32;
    } else {
#pragma unroll
      for (int m = 0; m < 4; ++m) rd(I0{}, I1{}, 0, m);
    }
#pragma unroll 1
    for (; j < G2_NKT - 1; ++j) tile(I1{}, I1{}, j);
    tile(I1{}, I2{}, G2_NKT - 1);
    asm volatile("s_nop 7\n\ts_nop 7" ::: "memory");
    SCHED();
  }
  STAMP(ws, 10);
  const int tile_id = rt * 8 + ct;
  char* slab_me = ws + WS_SLAB + (size_t)(tile_id * 2 + hs) * 32768;
  char* slab_pt = ws + WS_SLAB + (size_t)(tile_id * 2 + (hs ^ 1)) * 32768;
  GAS unsigned* flag_me = (GAS unsigned*)(ws + WS_FLAG) + (tile_id * 2 + hs) * 64;
  GAS unsigned* flag_pt = (GAS unsigned*)(ws + WS_FLAG) + (tile_id * 2 + (hs ^ 1)) * 64;
  asm volatile("s_waitcnt vmcnt(0) lgkmcnt(0)" ::: "memory");
  BAR();
  if (wid < 4) {
    if (wm != hs) {
      __amdgpu_buffer_rsrc_t rs = __builtin_amdgcn_make_buffer_rsrc(slab_me, 0, 32768, 0x00020000);
#pragma unroll
      for (int m = 0; m < 4; ++m)
#pragma unroll
        for (int n = 0; n < 4; ++n)
          __builtin_amdgcn_raw_buffer_store_b128(__builtin_bit_cast(u32x4, acc[m][n]), rs,
                                                 ((wn * 16 + m * 4 + n) * 64 + lane) * 16, 0, 16);
      asm volatile("s_waitcnt vmcnt(0)" ::: "memory");
    } else {
#pragma unroll
      for (int m = 0; m < 4; ++m)
#pragma unroll
        for (int n = 0; n < 4; ++n) *(LAS f32x4*)(lds + ((wn * 16 + m * 4 + n) * 64 + lane) * 16) = acc[m][n];
      asm volatile("s_waitcnt lgkmcnt(0)" ::: "memory");
    }
  }
  const int wn2 = wid >> 2, m2 = wid & 3;
  const int t = rt * 128 + hs * 64 + m2 * 16 + fr;
  const float g = ((const float*)(ws + WS_G))[t];
  u32x2 xv[4], dv[4];
#pragma unroll
  for (int n = 0; n < 4; ++n) {
    const int d = ct * 128 + wn2 * 64 + n * 16 + 4 * fq;
    xv[n] = *(const u32x2*)(x + (size_t)t * D_ + d);
    dv[n] = *(const u32x2*)(dskip + d);
  }
  asm volatile("" ::: "memory");
  __builtin_amdgcn_s_barrier();
  asm volatile("" ::: "memory");
  STAMP(ws, 11);
  if (tid == 0) __hip_atomic_store(flag_me, 1u, RLX_AGENT);
  if (wid == 4) {
    unsigned spins = 0;
    while ((unsigned)__builtin_amdgcn_readfirstlane(__hip_atomic_load(flag_pt, RLX_AGENT)) != 1u) {
      if (++spins > (1u << 24)) break;
      __builtin_amdgcn_s_sleep(1);
    }
  }
  asm volatile("" ::: "memory");
  __builtin_amdgcn_s_barrier();
  asm volatile("" ::: "memory");
  STAMP(ws, 12);
  {
    __amdgpu_buffer_rsrc_t rs = __builtin_amdgcn_make_buffer_rsrc(slab_pt, 0, 32768, 0x00020000);
    u32x4 pv[4];
#pragma unroll
    for (int n = 0; n < 4; ++n)
      pv[n] = __builtin_amdgcn_raw_buffer_load_b128(rs, ((wn2 * 16 + m2 * 4 + n) * 64 + lane) * 16, 0, 16);
#pragma unroll
    for (int n = 0; n < 4; ++n) {
      const int d = ct * 128 + wn2 * 64 + n * 16 + 4 * fq;
      f32x4 o = *(LAS f32x4*)(lds + ((wn2 * 16 + m2 * 4 + n) * 64 + lane) * 16) + __builtin_bit_cast(f32x4, pv[n]);
      o[0] += g * rbf(bf2f(xv[n][0] & 0xffffu) * bf2f(dv[n][0] & 0xffffu));
      o[1] += g * rbf(bf2f(xv[n][0] >> 16) * bf2f(dv[n][0] >> 16));
      o[2] += g * rbf(bf2f(xv[n][1] & 0xffffu) * bf2f(dv[n][1] & 0xffffu));
      o[3] += g * rbf(bf2f(xv[n][1] >> 16) * bf2f(dv[n][1] >> 16));
      *(f32x4*)(out + (size_t)t * D_ + d) = o;
    }
  }
  STAMP(ws, 13);
}

extern "C" void kernel_launch(void* const* d_in, const int* in_sizes, int n_in, void* d_out, int out_size,
                              void* d_ws, size_t ws_size, hipStream_t stream) {
  const u16* x = (const u16*)d_in[0];
  const u16* lre = (const u16*)d_in[1];
  const u16* lim = (const u16*)d_in[2];
  const u16* bre = (const u16*)d_in[3];
  const u16* bim = (const u16*)d_in[4];
  const u16* cre = (const u16*)d_in[5];
  const u16* cim = (const u16*)d_in[6];
  const u16* ldt = (const u16*)d_in[7];
  const u16* dsk = (const u16*)d_in[8];
  const u16* w1 = (const u16*)d_in[10];
  const u16* w3 = (const u16*)d_in[11];
  const u16* w2 = (const u16*)d_in[12];
  const u16* gw = (const u16*)d_in[13];
  char* ws = (char*)d_ws;
  hipFuncSetAttribute((const void*)k_gemm1, hipFuncAttributeMaxDynamicSharedMemorySize, G1_LDS);
  hipFuncSetAttribute((const void*)k_gemm2, hipFuncAttributeMaxDynamicSharedMemorySize, G2_LDS);
  k_prep<<<256, 512, 0, stream>>>(x, gw, lre, lim, ldt, ws, w1, w3, bre, bim, cre, cim, w2);
  k_gemm1<<<256, 512, G1_LDS, stream>>>(x, w1, w3, bre, bim, ws, cre, cim, w2);
  k_gemm2<<<256, 512, G2_LDS, stream>>>(x, cre, cim, w2, dsk, ws, (float*)d_out);
}
```

```cpp
#include <hip/hip_runtime.h>
#include <hip/hip_bf16.h>
#include <stdint.h>
#include <type_traits>

typedef unsigned short u16;
typedef unsigned long long u64;
typedef short bf16x8 __attribute__((ext_vector_type(8)));
typedef short s16x4 __attribute__((ext_vector_type(4)));
typedef float f32x4 __attribute__((ext_vector_type(4)));
typedef float f32x2 __attribute__((ext_vector_type(2)));
typedef unsigned u32x4 __attribute__((ext_vector_type(4)));
typedef unsigned u32x2 __attribute__((ext_vector_type(2)));
#define LAS __attribute__((address_space(3)))
#define GAS __attribute__((address_space(1)))
#define RLX_AGENT __ATOMIC_RELAXED, __HIP_MEMORY_SCOPE_AGENT

#define PFD 6
#define W2S 26
#define W2_128
constexpr int S_ = 2048, D_ = 1024, P_ = 1024, F_ = 3584, KT_ = 2 * P_ + F_;

constexpr size_t WS_GRAN = 0;
constexpr size_t WS_FLAG = 131072;
constexpr size_t WS_CNT  = WS_FLAG + 65536;
constexpr size_t WS_G    = WS_CNT + 2048;
constexpr size_t WS_TAB  = WS_G + 8192;
constexpr size_t WS_A2   = WS_TAB + 16384;
constexpr size_t WS_SLAB = WS_A2 + (size_t)S_ * KT_ * 2;

#define STAMP(ws, k) do {} while (0)
__device__ __forceinline__ float bf2f(unsigned v16) { return __uint_as_float(v16 << 16); }
__device__ __forceinline__ unsigned cvt_pk_bf16(float lo, float hi) {
  unsigned r; asm("v_cvt_pk_bf16_f32 %0, %1, %2" : "=v"(r) : "v"(lo), "v"(hi)); return r;
}
__device__ __forceinline__ float rbf(float f) { return __uint_as_float(cvt_pk_bf16(f, f) << 16); }

__global__ __launch_bounds__(512) void k_prep(const u16* __restrict__ x, const u16* __restrict__ gw,
                                              const u16* __restrict__ lre, const u16* __restrict__ lim,
                                              const u16* __restrict__ ldt, char* __restrict__ ws,
                                              const u16* __restrict__ w1, const u16* __restrict__ w3,
                                              const u16* __restrict__ bre, const u16* __restrict__ bim,
                                              const u16* __restrict__ cre, const u16* __restrict__ cim,
                                              const u16* __restrict__ w2) {
  const int tid = threadIdx.x, wid = tid >> 6, lane = tid & 63, b = blockIdx.x;
  const int pch = b * 4 + (tid & 3);
  const unsigned in_lr = lre[pch], in_li = lim[pch], in_dt = ldt[pch];
  u32x4 wacc = {0u, 0u, 0u, 0u};
  {
    const int xg = b & 7, j32 = b >> 3;
    constexpr int CPR = PFD * 4;
    constexpr int NX = (64 * CPR + 511) / 512, NW = (PFD * 2 * 56 + 511) / 512;
    u32x4 v[NX + NW + 1];
#pragma unroll
    for (int i = 0; i < NX; ++i) {
      int c = tid + i * 512; c = c < 64 * CPR ? c : 0;
      v[i] = *(const u32x4*)(x + (size_t)(64 * j32 + c / CPR) * D_ + (c % CPR) * 8);
    }
#pragma unroll
    for (int i = 0; i < NW; ++i) {
      int c = tid + i * 512; c = c < PFD * 2 * 56 ? c : 0;
      const int mat = c / (PFD * 56), r = c % (PFD * 56);
      v[NX + i] = *(const u32x4*)((mat ? w3 : w1) + (size_t)(j32 * PFD + r / 56) * F_ + xg * 448 + (r % 56) * 8);
    }
    {
      const int c = tid < 8 * CPR ? tid : 0, r = c / CPR;
      v[NX + NW] = *(const u32x4*)((r < 4 ? bre : bim) + (size_t)(128 * xg + 4 * j32 + (r & 3)) * D_ + (c % CPR) * 8);
    }
#pragma unroll
    for (int i = 0; i < NX + NW + 1; ++i) wacc ^= v[i];
  }
  if (tid < 64) ((u64*)(ws + WS_GRAN))[b * 64 + tid] = 0ull;
  if (tid == 64) ((unsigned*)(ws + WS_FLAG))[b * 64] = 0u;
  if (tid == 65 && b < 8) ((unsigned*)(ws + WS_CNT))[b * 64] = 0u;
  const int row = b * 8 + wid;
  const u32x4* xp = (const u32x4*)(x + (size_t)row * D_);
  const u32x4* gp = (const u32x4*)gw;
  float s = 0.f;
#pragma unroll
  for (int i = 0; i < 2; ++i) {
    u32x4 a = xp[lane + 64 * i], c = gp[lane + 64 * i];
#pragma unroll
    for (int j = 0; j < 4; ++j) {
      s += rbf(bf2f(a[j] & 0xffffu) * bf2f(c[j] & 0xffffu));
      s += rbf(bf2f(a[j] >> 16) * bf2f(c[j] >> 16));
    }
  }
#pragma unroll
  for (int o = 32; o >= 1; o >>= 1) s += __shfl_xor(s, o);
  if (lane == 0) {
    s = rbf(s);
    float g = rbf(1.0f / (1.0f + expf(-s)));
    ((float*)(ws + WS_G))[row] = g;
  }
  asm volatile("" :: "v"(wacc));
  if (tid >= 448 && tid < 452) {
    const int p = pch;
    const float lr = bf2f(in_lr), li = bf2f(in_li);
    const float dt = rbf(expf(bf2f(in_dt)));
    const float zr = dt * lr, zi = dt * li;
    const float e = expf(zr);
    float sn, cs; sincosf(zi, &sn, &cs);
    const float br = e * cs, bi = e * sn;
    const float nr = br - 1.0f, ni = bi;
    const float den = lr * lr + li * li;
    const float cr = (nr * lr + ni * li) / den, ci = (ni * lr - nr * li) / den;
    ((f32x4*)(ws + WS_TAB))[p] = (f32x4){br, bi, cr, ci};
  }
}

constexpr int G1_A = 0;
constexpr int G1_B = 65536;
constexpr int G1_W = 81920;
constexpr int G1_RING = 147456;
constexpr int G1_LDS = G1_RING + 8192;

typedef __amdgpu_buffer_rsrc_t rsrc_t;
__device__ __forceinline__ rsrc_t mk_rsrc(const void* p, unsigned bytes) {
  return __builtin_amdgcn_make_buffer_rsrc((void*)p, 0, bytes, 0x00020000);
}
__device__ __forceinline__ void dma16(rsrc_t rs, LAS char* dst, unsigned voff, unsigned soff) {
  asm volatile("s_mov_b32 m0, %0\n\ts_nop 0\n\tbuffer_load_dwordx4 %1, %2, %3 offen lds"
               :: "s"((unsigned)(size_t)dst), "v"(voff), "s"(rs), "s"(soff) : "memory");
}
__device__ __forceinline__ void touch4(rsrc_t rs, LAS char* dst, unsigned voff, unsigned soff) {
  soff = __builtin_amdgcn_readfirstlane(soff);
  asm volatile("" : "+s"(soff));
  asm volatile("s_mov_b32 m0, %0\n\ts_nop 0\n\tbuffer_load_dword %1, %2, %3 offen lds"
               :: "s"((unsigned)(size_t)dst), "v"(voff), "s"(rs), "s"(soff) : "memory");
}
__device__ __forceinline__ void touchg(const char* p, LAS char* dst) {
  asm volatile("s_mov_b32 m0, %0\n\ts_nop 0\n\tglobal_load_lds_dword %1, off" :: "s"((unsigned)(size_t)dst), "v"(p) : "memory");
}
__device__ __forceinline__ bf16x8 lds_b128(LAS char* p) { return *(LAS bf16x8*)p; }
__device__ __forceinline__ bf16x8 lds_tr2(LAS char* p0, LAS char* p1) {
  s16x4 a = __builtin_amdgcn_ds_read_tr16_b64_v4i16((LAS s16x4*)p0);
  s16x4 b = __builtin_amdgcn_ds_read_tr16_b64_v4i16((LAS s16x4*)p1);
  return __builtin_shufflevector(a, b, 0, 1, 2, 3, 4, 5, 6, 7);
}
#define MFMA(a, b, c) __builtin_amdgcn_mfma_f32_16x16x32_bf16((a), (b), (c), 0, 0, 0)
#define MFMA_IP(c, a, b) asm volatile("v_mfma_f32_16x16x32_bf16 %0, %1, %2, %0" : "+v"(c) : "v"(a), "v"(b))

#define ST_SC1
#define A2_STORE(p, v) asm volatile("global_store_dwordx4 %0, %1, off sc1\n\ts_nop 1" :: "v"(p), "v"(v) : "memory")
__device__ __forceinline__ u16* a2_chunk(u16* A2, int row, int kc) {
  return A2 + ((size_t)((row >> 5) * (KT_ / 16) + (kc >> 1)) * 64 + (kc & 1) * 32 + (row & 31)) * 8;
}
typedef float f32x16 __attribute__((ext_vector_type(16)));
#define MFMA32_IP(c, a, b) asm volatile("v_mfma_f32_32x32x16_bf16 %0, %1, %2, %0" : "+v"(c) : "v"(a), "v"(b))
#define SCHED() __builtin_amdgcn_sched_barrier(0)
#define BAR() do { SCHED(); __builtin_amdgcn_s_barrier(); SCHED(); } while (0)

struct G1Stage {
  rsrc_t rx, rw, r4;
  unsigned voE, voO;
  unsigned voW, voW2, vo4, vo5;
  unsigned so4, ld4;
  unsigned bs4;
  const char* pf0; const char* pf1; unsigned pfs1;
  char* wsdbg;
  const char *c0, *c1, *c2;
};

template <int ROLE>
__device__ __forceinline__ void gemm1_body(LAS char* lds, G1Stage& st, int wid, int wm, int lane, f32x4 (&acc)[4][9]) {
  constexpr int WN = ROLE == 0 ? 0 : 1;
  constexpr int NG = WN == 0 ? 4 : 3;
  constexpr int S0 = WN == 0 ? 0 : 1;
  constexpr int NA = ROLE == 2 ? 4 : 6, NW = ROLE == 2 ? 4 : 2, HA = NA / 2;
  const int fr = lane & 15, fq = lane >> 4;
  const int frag = fr * 128 + ((fq ^ (fr >> 1)) << 4);
  const int a_off = G1_A + wm * 8192 + frag;
  const int s_off = G1_B + S0 * 2048 + frag;
  const int q = (lane & 15) >> 2, p = lane & 3;
  const int w_base = G1_W + WN * 4096 + (8 * fq + q) * 128 + ((((q >> 1) & 1) | ((fq & 1) << 1)) << 5) + p * 8;

  auto apiece = [&](int k, int pr) {
    if (ROLE < 3 && k < NA) {
      const unsigned vo = (k & 1) ? st.voO : st.voE;
      if (k < 4) {
        const int ap = 14 * (k >> 1) + 2 * wid + (k & 1);
        dma16(st.rx, lds + G1_A + (pr & 1) * 32768 + ap * 1024, vo, pr * 128 + ap * 16384);
      } else {
        dma16(st.r4, lds + st.ld4 + (pr & 1) * st.bs4 + (k & 1) * 1024, vo, pr * 128 + st.so4 + (k & 1) * 16384);
      }
    }
  };
  auto wpiece = [&](int k, int j) {
    if (ROLE < 3 && k < NW) {
      LAS char* d = lds + G1_W + (j & 3) * 16384;
      if (k == 0) dma16(st.rw, d + (wid >> 2) * 8192 + (wid & 3) * 1024, st.voW, j * (32 * F_ * 2));
      if (k == 1) dma16(st.rw, d + (wid >> 2) * 8192 + (wid & 3) * 1024 + 4096, st.voW2, j * (32 * F_ * 2));
      if (k == 2) dma16(st.rw, d + 8192 + 3 * 1024, st.vo4, j * (32 * F_ * 2));
      if (k == 3) dma16(st.rw, d + 8192 + 3 * 1024 + 4096, st.vo5, j * (32 * F_ * 2));
    }
  };
  auto step = [&](auto J4, auto TAILT, int j) {
    constexpr int j4 = decltype(J4)::value, TAIL = decltype(TAILT)::value;
    constexpr bool odd = j4 & 1, issue = TAIL == 0;
    const int pr = j >> 1;
    LAS char* ab = lds + ((j4 >> 1) & 1) * 32768;
    LAS char* bb = lds + ((j4 >> 1) & 1) * 8192;
    LAS char* wb = lds + j4 * 16384;
    bf16x8 xa[4], fb[9];
    if (ROLE == 0) {
      if (issue && !odd) apiece(0, pr + 1);
      if (odd && TAIL <= 29) wpiece(0, j + 2);
    } else if (issue && !odd) apiece(HA, pr + 1);
#pragma unroll
    for (int m = 0; m < 4; ++m) xa[m] = lds_b128(ab + ((a_off + m * 2048) ^ (odd ? 64 : 0)));
#pragma unroll
    for (int i = 0; i < NG; ++i) {
      fb[i] = lds_tr2(wb + (w_base ^ (i << 5)), wb + (w_base ^ (i << 5)) + 512);
      fb[4 + i] = lds_tr2(wb + (w_base ^ (i << 5)) + 8192, wb + (w_base ^ (i << 5)) + 512 + 8192);
    }
    if (WN == 0) fb[8] = lds_b128(bb + (s_off ^ (odd ? 64 : 0)));
    else {
      fb[3] = lds_b128(bb + (s_off ^ (odd ? 64 : 0)));
      fb[7] = lds_b128(bb + ((s_off + 2048) ^ (odd ? 64 : 0)));
      fb[8] = lds_b128(bb + ((s_off + 4096) ^ (odd ? 64 : 0)));
    }
    if (ROLE == 3) {
      if (j + PFD < 32) {
        if (!odd) { touchg(st.pf0, lds + G1_RING); st.pf0 += 128; }
        else { touchg(st.pf1, lds + G1_RING + 256); st.pf1 += st.pfs1; }
      }
      if (j >= W2S && j < W2S + 6) {
        const int jj = j - W2S;
        const char* pb = jj == 0 ? st.c0 : (jj == 1 ? st.c1 : st.c2 + (jj - 2) * 8192);
        touchg(pb + ((jj == 5 ? lane & 31 : lane) * 128), lds + G1_RING + 512);
      }
      asm volatile("s_waitcnt lgkmcnt(0)" ::: "memory");
    } else if (ROLE == 0) {
      if (issue && !odd) { apiece(1, pr + 1); apiece(2, pr + 1); }
      if (odd && TAIL <= 29) wpiece(1, j + 2);
      asm volatile("s_waitcnt lgkmcnt(0)" ::: "memory");
    } else {
      if (issue && !odd) {
#pragma unroll
        for (int k = HA + 1; k < NA; ++k) apiece(k, pr + 1);
      }
      if (odd && TAIL <= 29) {
#pragma unroll
        for (int k = 0; k < NW; ++k) wpiece(k, j + 2);
      }
      if (odd && issue) {
#pragma unroll
        for (int k = 0; k < NW; ++k) wpiece(k, j + 3);
      }
      constexpr int vm = TAIL == 0 ? (odd ? 2 * NW : NW + NA) : (TAIL == 29 ? NW : 0);
      asm volatile("s_waitcnt vmcnt(%0) lgkmcnt(0)" :: "n"(vm) : "memory");
    }
    BAR();
    __builtin_amdgcn_s_setprio(1);
#pragma unroll
    for (int i = 0; i < 9; ++i) {
#pragma unroll
      for (int m = 0; m < 4; ++m) MFMA_IP(acc[m][i], fb[i], xa[m]);
      if (issue) {
        if (ROLE == 0) {
          if (!odd) {
            if (i == 1) apiece(3, pr + 1);
            if (i == 3) apiece(4, pr + 1);
            if (i == 5) apiece(5, pr + 1);
          } else {
            if (i == 2) wpiece(0, j + 3);
            if (i == 5) wpiece(1, j + 3);
          }
        } else if (ROLE < 3 && odd && j < 29) {
          if (i == 1) apiece(0, pr + 2);
          if (i == 3) apiece(1, pr + 2);
          if (i == 5 && HA > 2) apiece(2, pr + 2);
        }
      }
    }
    __builtin_amdgcn_s_setprio(0);
    if (ROLE == 0) {
      constexpr int vm = TAIL == 0 ? (odd ? 4 : 8) : (TAIL == 29 ? 2 : 0);
      asm volatile("s_waitcnt vmcnt(%0)" :: "n"(vm) : "memory");
    }
    BAR();
  };
  using I0 = std::integral_constant<int, 0>; using I1 = std::integral_constant<int, 1>;
  using I2 = std::integral_constant<int, 2>; using I3 = std::integral_constant<int, 3>;

#pragma unroll
  for (int k = 0; k < 6; ++k) apiece(k, 0);
#pragma unroll
  for (int j = 0; j < 3; ++j)
#pragma unroll
    for (int k = 0; k < 4; ++k) wpiece(k, j);
  if (ROLE == 1 || ROLE == 2) {
#pragma unroll
    for (int k = 0; k < HA; ++k) apiece(k, 1);
  }
  if (ROLE == 3) {
    dma16(st.rx, lds + G1_RING + 1024, (unsigned)lane * 16u, 0u);
    dma16(st.r4, lds + G1_RING + 2048, (unsigned)lane * 16u, 0u);
  } else {
    asm volatile("s_waitcnt vmcnt(%0)" :: "n"(ROLE == 0 ? 2 * NW : 2 * NW + HA) : "memory");
  }
  BAR();
  if (WN == 1) BAR();
#pragma unroll 1
  for (int j = 0; j < 28; j += 4) {
    step(I0{}, I0{}, j); step(I1{}, I0{}, j + 1); step(I2{}, I0{}, j + 2); step(I3{}, I0{}, j + 3);
  }
  step(I0{}, I0{}, 28);
  step(I1{}, std::integral_constant<int, 29>{}, 29);
  step(I2{}, std::integral_constant<int, 30>{}, 30);
  step(I3{}, std::integral_constant<int, 31>{}, 31);
  if (WN == 0) BAR();
  if (ROLE == 3) asm volatile("s_waitcnt vmcnt(11)\n\ts_nop 7\n\ts_nop 7" ::: "memory");
  else asm volatile("s_waitcnt vmcnt(0)\n\ts_nop 7\n\ts_nop 7" ::: "memory");
  SCHED();
}

__device__ __forceinline__ int glu_blk(int l0, int c4, int nb) {
  const int full = 8 * c4;
  const int lo = 7 * c4;
  const int r = nb - 4, pre = full - lo;
  const int b = nb < 4 ? full + nb : (r < pre ? lo + r : full + 4 + (r - pre));
  return l0 * 4 + b;
}

__device__ __forceinline__ void phase1(LAS char* lds, const u16* __restrict__ x, const u16* __restrict__ w1,
                                       const u16* __restrict__ w3, const u16* __restrict__ bre,
                                       const u16* __restrict__ bim, char* __restrict__ ws,
                                       const u16* __restrict__ cre, const u16* __restrict__ cim,
                                       const u16* __restrict__ w2) {
  const int tid = threadIdx.x, lane = tid & 63;
  const int wid = __builtin_amdgcn_readfirstlane(tid >> 6);
  const int wm = wid & 3, wn = wid >> 2;
  const int bid = blockIdx.x;
  const int cb = (bid & 7) * 4 + ((bid >> 3) & 3), rb = bid >> 5;
  STAMP(ws, 0);

  G1Stage st;
  {
    const int r = lane >> 3;
    st.voE = (unsigned)(r * D_ + (((lane & 7) ^ (r >> 1)) * 8)) * 2u;
    st.voO = (unsigned)(r * D_ + (((lane & 7) ^ (4 + (r >> 1))) * 8)) * 2u;
  }
  const int c4 = (bid >> 3) & 3, l0 = (bid & 7) * 7;
  {
    const int r = lane >> 3, pos = lane & 7;
    auto woff = [&](int kb, int half) -> unsigned {
      const int k = 8 * kb + r, f = ((k >> 1) & 1) | (((k >> 3) & 1) << 1), b = (pos >> 1) ^ f;
      const int blk = glu_blk(l0, c4, half * 4 + (b < 3 || half == 0 ? b : 0));
      return (half == 1 && b == 3) ? 0x80000000u : (unsigned)(k * F_ + blk * 16 + (pos & 1) * 8) * 2u;
    };
    st.voW = woff(wid & 3, 0); st.voW2 = woff(wid & 3, 1);
    st.vo4 = woff(3, 0); st.vo5 = woff(3, 1);
  }
  st.rx = mk_rsrc(x + (size_t)rb * 256 * D_, 256u * D_ * 2u);
  st.rw = mk_rsrc(wn ? w3 : w1, (unsigned)(D_ * F_) * 2u);
  if (wid < 2) {
    st.r4 = st.rx; st.so4 = (unsigned)(28 + 2 * wid) * 16384u; st.ld4 = G1_A + (28 + 2 * wid) * 1024; st.bs4 = 32768u;
  } else {
    const int bp = (2 * wid - 4) & 7;
    st.r4 = mk_rsrc((bp < 4 ? bre : bim) + (size_t)cb * 32 * D_, 32u * D_ * 2u);
    st.so4 = (unsigned)(bp & 3) * 16384u; st.ld4 = G1_B + bp * 1024; st.bs4 = 8192u;
  }
  if (wid == 7) {
    st.rx = mk_rsrc(ws + WS_G + rb * 1024, 1024u);
    st.r4 = mk_rsrc(ws + WS_TAB + cb * 512, 512u);
  }
  {
    const int xg = bid & 7, j32 = bid >> 3;
    st.pf0 = (const char*)(x + (size_t)(64 * j32 + lane) * D_) + PFD * 64;
    if (lane < 28) {
      const int l = lane % 14;
      st.pf1 = (const char*)((lane < 14 ? w1 : w3) + (size_t)(PFD * 32 + 2 * j32 + l / 7) * F_ + xg * 448) + (l % 7) * 128;
      st.pfs1 = 64u * F_ * 2u;
    } else {
      const int l = (lane - 28) & 7;
      st.pf1 = (const char*)((l < 4 ? bre : bim) + (size_t)(128 * xg + 4 * j32 + (l & 3)) * D_) + PFD * 64;
      st.pfs1 = 128u;
    }
    st.wsdbg = ws + (210u << 20);
    st.c0 = (const char*)cre + (size_t)bid * 8192;
    st.c1 = (const char*)cim + (size_t)bid * 8192;
    st.c2 = (const char*)w2 + (size_t)bid * 28672;
  }

  f32x4 acc[4][9];
#pragma unroll
  for (int m = 0; m < 4; ++m)
#pragma unroll
    for (int i = 0; i < 9; ++i) acc[m][i] = (f32x4){0, 0, 0, 0};
  if (wid < 4)       gemm1_body<0>(lds, st, wid, wm, lane, acc);
  else if (wid < 6)  gemm1_body<1>(lds, st, wid, wm, lane, acc);
  else if (wid == 6) gemm1_body<2>(lds, st, wid, wm, lane, acc);
  else               gemm1_body<3>(lds, st, wid, wm, lane, acc);

  STAMP(ws, 1);
  const int fr = lane & 15, fq = lane >> 4;
  LAS float* U = (LAS float*)lds;
#pragma unroll
  for (int m = 0; m < 4; ++m) {
    const int t = wm * 64 + m * 16 + fr;
    LAS float* u = U + t * 68 + (t >> 4) * 4 + 4 * fq;
    if (wn == 0) {
      *(LAS f32x4*)(u) = acc[m][8];
    } else {
      *(LAS f32x4*)(u + 16) = acc[m][3];
      *(LAS f32x4*)(u + 32) = acc[m][7];
      *(LAS f32x4*)(u + 48) = acc[m][8];
    }
  }
  const int p = wid * 4 + fq, seg = fr;
  asm volatile("s_waitcnt lgkmcnt(0)" ::: "memory");
  __builtin_amdgcn_s_barrier();
  asm volatile("" ::: "memory");
  const f32x4 tb = ((LAS f32x4*)(lds + G1_RING + 2048))[p];
  LAS float* gv = (LAS float*)(lds + G1_RING + 1024);
  const float lr = tb[0], li = tb[1];
  float sr[16], si[16];
#pragma unroll
  for (int i = 0; i < 16; ++i) {
    sr[i] = U[(seg * 16 + i) * 68 + seg * 4 + p];
    si[i] = U[(seg * 16 + i) * 68 + seg * 4 + 32 + p];
  }
#pragma unroll
  for (int i = 1; i < 16; ++i) {
    const float a = sr[i - 1], b = si[i - 1];
    sr[i] += lr * a - li * b;
    si[i] += lr * b + li * a;
  }
  float pr_ = lr, pi_ = li;
#pragma unroll
  for (int i = 0; i < 4; ++i) { const float a = pr_ * pr_ - pi_ * pi_, b = 2.f * pr_ * pi_; pr_ = a; pi_ = b; }
  float tr_ = sr[15], ti_ = si[15];
  float lsr = 1.f, lsi = 0.f;
#pragma unroll
  for (int d = 1; d < 16; d <<= 1) {
    const float ar = __shfl_up(tr_, d, 16), ai = __shfl_up(ti_, d, 16);
    if (seg >= d) { tr_ += pr_ * ar - pi_ * ai; ti_ += pr_ * ai + pi_ * ar; }
    if (seg & d) { const float a = lsr * pr_ - lsi * pi_, b = lsr * pi_ + lsi * pr_; lsr = a; lsi = b; }
    const float a = pr_ * pr_ - pi_ * pi_, b = 2.f * pr_ * pi_; pr_ = a; pi_ = b;
  }
  GAS u64* gran = (GAS u64*)(ws + WS_GRAN) + (size_t)cb * 8 * 64;
  if (seg == 15 && rb < 7) {
    __hip_atomic_store(gran + rb * 64 + p, (1ull << 32) | (u64)__float_as_uint(tr_), RLX_AGENT);
    __hip_atomic_store(gran + rb * 64 + 32 + p, (1ull << 32) | (u64)__float_as_uint(ti_), RLX_AGENT);
  }
  float cr = __shfl_up(tr_, 1, 16), ci = __shfl_up(ti_, 1, 16);
  if (seg == 0) { cr = 0.f; ci = 0.f; }
  STAMP(ws, 2);
  u16* A2 = (u16*)(ws + WS_A2);
  {
    LAS char* HT = lds + 73728;
    const int ng = wn == 0 ? 4 : 3;
#pragma unroll
    for (int m = 0; m < 4; ++m) {
      const int tl = wm * 64 + m * 16 + fr;
      const float omg = 1.0f - gv[tl];
#pragma unroll
      for (int i = 0; i < 4; ++i) {
        if (i < ng) {
          const f32x4 a = acc[m][i];
          const f32x4 au = a * acc[m][4 + i] * omg;
          const f32x4 z = a * -1.44269504089f;
          f32x4 e;
#pragma unroll
          for (int j = 0; j < 4; ++j) e[j] = __builtin_amdgcn_exp2f(z[j]);
          e = e + 1.0f;
          f32x4 r;
#pragma unroll
          for (int j = 0; j < 4; ++j) r[j] = __builtin_amdgcn_rcpf(e[j]);
          const f32x4 h = au * r;
          *(LAS u32x2*)(HT + tl * 240 + (wn * 4 + i) * 32 + fq * 8) = (u32x2){cvt_pk_bf16(h[0], h[1]), cvt_pk_bf16(h[2], h[3])};
        }
      }
    }
    asm volatile("s_waitcnt lgkmcnt(0)" ::: "memory");
    __builtin_amdgcn_s_barrier();
    asm volatile("" ::: "memory");
#pragma unroll
    for (int i = 0; i < 7; ++i) {
      const int idx = tid + 512 * i, u = idx >> 5, c = u % 14, t = (u / 14) * 32 + (idx & 31), nb = c >> 1;
      const int blk = glu_blk(l0, c4, nb);
      const u32x4 v = *(LAS u32x4*)(HT + t * 240 + c * 16);
      A2_STORE(a2_chunk(A2, rb * 256 + t, 256 + blk * 2 + (c & 1)), v);
    }
  }
  STAMP(ws, 3);
  float hr = 0.f, hi = 0.f;
  if (seg < rb) {
    u64 v0, v1; unsigned spins = 0;
    for (;;) {
      v0 = __hip_atomic_load(gran + seg * 64 + p, RLX_AGENT);
      v1 = __hip_atomic_load(gran + seg * 64 + 32 + p, RLX_AGENT);
      if ((v0 >> 32) == 1ull && (v1 >> 32) == 1ull) break;
      if (++spins > (1u << 24)) break;
      __builtin_amdgcn_s_sleep(1);
    }
    hr = __uint_as_float((unsigned)v0); hi = __uint_as_float((unsigned)v1);
    for (int e = rb - 1 - seg; e > 0; --e) { const float a = pr_ * hr - pi_ * hi, b = pr_ * hi + pi_ * hr; hr = a; hi = b; }
  }
#pragma unroll
  for (int d = 1; d < 8; d <<= 1) { hr += __shfl_xor(hr, d, 16); hi += __shfl_xor(hi, d, 16); }
  hr = __shfl(hr, 0, 16); hi = __shfl(hi, 0, 16);
  STAMP(ws, 4);
  cr += lsr * hr - lsi * hi;
  ci += lsr * hi + lsi * hr;
  LAS char* HS = lds;
  const float cfr = tb[2], cfi = tb[3];
#pragma unroll
  for (int i = 0; i < 16; ++i) {
    const float nr = lr * cr - li * ci, ni = lr * ci + li * cr;
    cr = nr; ci = ni;
    const float a = sr[i] + cr, b = si[i] + ci;
    const float g2 = 2.0f * gv[seg * 16 + i];
    const float orr = (cfr * a - cfi * b) * g2, oi = -(cfr * b + cfi * a) * g2;
    const unsigned pk = cvt_pk_bf16(orr, oi);
    LAS u16* hp = (LAS u16*)(HS + (seg * 16 + i) * 128 + seg * 16) + p;
    hp[0] = (u16)(pk & 0xffffu);
    hp[32] = (u16)(pk >> 16);
  }
  asm volatile("s_waitcnt lgkmcnt(0)" ::: "memory");
  __builtin_amdgcn_s_barrier();
  asm volatile("" ::: "memory");
#pragma unroll
  for (int i = 0; i < 4; ++i) {
    const int idx = tid + 512 * i, c = (idx >> 5) & 7, t = (idx >> 8) * 32 + (idx & 31);
    const u32x4 v = *(LAS u32x4*)(HS + t * 128 + c * 16 + (t >> 4) * 16);
    A2_STORE(a2_chunk(A2, rb * 256 + t, (c >> 2) * 128 + cb * 4 + (c & 3)), v);
  }
  STAMP(ws, 5);
  asm volatile("s_waitcnt vmcnt(0) lgkmcnt(0)" ::: "memory");
  __builtin_amdgcn_s_barrier();
  asm volatile("" ::: "memory");
  if (tid == 0) __hip_atomic_fetch_add((GAS unsigned*)(ws + WS_CNT) + rb * 64, 1u, RLX_AGENT);
}

constexpr int G2_STAGE = 16384, G2_NBUF = 8, G2_LDS = G2_STAGE * G2_NBUF;
constexpr int G2_NKT = 44;

__device__ __forceinline__ void phase2(LAS char* lds, const u16* __restrict__ x, const u16* __restrict__ cre,
                                       const u16* __restrict__ cim, const u16* __restrict__ w2,
                                       const u16* __restrict__ dskip, char* __restrict__ ws,
                                       float* __restrict__ out) {
  const int tid = threadIdx.x, lane = tid & 63;
  const int wid = __builtin_amdgcn_readfirstlane(tid >> 6);
  const int bid = blockIdx.x, xcd = bid & 7, idx = bid >> 3;
  const int rt = (xcd >> 1) * 4 + (idx & 3), ct = (xcd & 1) * 4 + ((idx >> 2) & 3), hs = idx >> 4;
  const int fr = lane & 15, fq = lane >> 4;
  const int kt0 = hs * G2_NKT;
  STAMP(ws, 8);
  f32x16 acc[4];
#pragma unroll
  for (int n = 0; n < 4; ++n)
#pragma unroll
    for (int i = 0; i < 16; ++i) acc[n][i] = 0.f;

  if (wid >= 4) {
    const int l = wid - 4;
    const int arow = 8 * l + (lane >> 3);
    const unsigned voC = (unsigned)(arow * P_ + ((lane & 7) ^ ((arow >> 1) & 7)) * 8) * 2u;
    const unsigned voW = (unsigned)(arow * D_ + ((((lane & 7) >> 1) ^ (arow & 2)) * 16) + (lane & 1) * 8) * 2u;
    const rsrc_t rcr = mk_rsrc(cre + (size_t)ct * 128 * P_, 128u * P_ * 2u);
    const rsrc_t rci = mk_rsrc(cim + (size_t)ct * 128 * P_, 128u * P_ * 2u);
    const rsrc_t rw = mk_rsrc(w2 + ct * 128, (unsigned)(F_ * D_ - ct * 128) * 2u);
    auto stage = [&](int buf, int ktg) {
      LAS char* d = lds + buf * G2_STAGE + l * 1024;
      if (ktg < 32) {
        const rsrc_t rc = ktg < 16 ? rcr : rci;
#pragma unroll
        for (int i = 0; i < 4; ++i) dma16(rc, d + i * 4096, voC, (ktg & 15) * 128 + i * (32 * P_ * 2));
      } else {
#pragma unroll
        for (int i = 0; i < 4; ++i)
          dma16(rw, d + i * 4096, voW, (ktg - 32) * (64 * D_ * 2) + (i & 1) * (32 * D_ * 2) + (i >> 1) * 128);
      }
    };
#pragma unroll
    for (int t = 0; t < 8; ++t) stage(t, kt0 + t);
    asm volatile("s_waitcnt vmcnt(28)" ::: "memory");
    BAR();
#pragma unroll 1
    for (int j = 0; j < G2_NKT - 8; ++j) {
      asm volatile("s_waitcnt vmcnt(24)" ::: "memory");
      BAR();
      stage(j & 7, kt0 + j + 8);
    }
    asm volatile("s_waitcnt vmcnt(24)" ::: "memory"); BAR();
    asm volatile("s_waitcnt vmcnt(20)" ::: "memory"); BAR();
    asm volatile("s_waitcnt vmcnt(16)" ::: "memory"); BAR();
    asm volatile("s_waitcnt vmcnt(12)" ::: "memory"); BAR();
    asm volatile("s_waitcnt vmcnt(8)" ::: "memory"); BAR();
    asm volatile("s_waitcnt vmcnt(4)" ::: "memory"); BAR();
    asm volatile("s_waitcnt vmcnt(0)" ::: "memory"); BAR();
  } else {
    const int r32 = lane & 31, h = lane >> 5;
    const int c_off = r32 * 128 + ((h ^ ((r32 >> 1) & 7)) << 4);
    const int q = (lane & 15) >> 2, p = lane & 3;
    const int w_base = (8 * h + q) * 128 + ((((lane >> 4) & 1) ^ (q & 2)) << 5) + p * 8;
    const rsrc_t ra = mk_rsrc(ws + WS_A2 + (size_t)(rt * 4 + wid) * (KT_ / 16) * 1024, (unsigned)(KT_ / 16) * 1024u);
    const unsigned voA = (unsigned)lane * 16u;
    bf16x8 fa[4][4];
    bf16x8 fb[2][4];
    auto lda1 = [&](auto SL, int j, int s2) {
      constexpr int sl = decltype(SL)::value;
      const int jj = j < G2_NKT ? j : G2_NKT - 1;
      fa[sl][s2] = __builtin_bit_cast(bf16x8, __builtin_amdgcn_raw_buffer_load_b128(ra, voA, ((kt0 + jj) * 4 + s2) * 1024, 16));
    };
    auto rd = [&](auto TYPE, int slot, int s2, int n) {
      constexpr int ty = decltype(TYPE)::value;
      LAS char* base = lds + slot * G2_STAGE;
      if (ty == 0) fb[s2 & 1][n] = lds_b128(base + ((c_off + n * 4096) ^ (s2 << 5)));
      else {
        LAS char* pp = base + ((w_base + (n >> 1) * 8192) ^ ((n & 1) << 6)) + s2 * 2048;
        fb[s2 & 1][n] = lds_tr2(pp, pp + 512);
      }
    };
    using I0 = std::integral_constant<int, 0>; using I1 = std::integral_constant<int, 1>;
    using I2 = std::integral_constant<int, 2>; using I3 = std::integral_constant<int, 3>;
    auto tile = [&](auto SL, auto TY, auto NT, int j) {
      constexpr int nt = decltype(NT)::value, sl = decltype(SL)::value;
#pragma unroll
      for (int s2 = 0; s2 < 4; ++s2) {
        if (s2 == 3) {
          asm volatile("s_waitcnt lgkmcnt(0)" ::: "memory");
          if (nt < 2) BAR();
        }
#pragma unroll
        for (int n = 0; n < 4; ++n) {
          if (s2 < 3) rd(TY, j & 7, s2 + 1, n);
          else if (nt == 0) rd(I0{}, (j + 1) & 7, 0, n);
          else if (nt == 1) rd(I1{}, (j + 1) & 7, 0, n);
          MFMA32_IP(acc[n], fb[s2 & 1][n], fa[sl][s2]);
          SCHED();
        }
        lda1(SL, j + 4, s2);
        SCHED();
      }
    };
    {
      GAS unsigned* cnt = (GAS unsigned*)(ws + WS_CNT) + (rt >> 1) * 64;
      unsigned spins = 0;
      while ((unsigned)__builtin_amdgcn_readfirstlane(__hip_atomic_load(cnt, RLX_AGENT)) < 32u) {
        if (++spins > (1u << 24)) break;
        __builtin_amdgcn_s_sleep(1);
      }
      asm volatile("" ::: "memory");
    }
#pragma unroll
    for (int s2 = 0; s2 < 4; ++s2) { lda1(I0{}, 0, s2); lda1(I1{}, 1, s2); lda1(I2{}, 2, s2); lda1(I3{}, 3, s2); }
    BAR();
    STAMP(ws, 9);
    int j = 0;
    if (hs == 0) {
#pragma unroll
      for (int n = 0; n < 4; ++n) rd(I0{}, 0, 0, n);
#pragma unroll 1
      for (; j < 28; j += 4) {
        tile(I0{}, I0{}, I0{}, j); tile(I1{}, I0{}, I0{}, j + 1); tile(I2{}, I0{}, I0{}, j + 2); tile(I3{}, I0{}, I0{}, j + 3);
      }
      tile(I0{}, I0{}, I0{}, 28); tile(I1{}, I0{}, I0{}, 29); tile(I2{}, I0{}, I0{}, 30); tile(I3{}, I0{}, I1{}, 31);
      j = 32;
    } else {
#pragma unroll
      for (int n = 0; n < 4; ++n) rd(I1{}, 0, 0, n);
    }
#pragma unroll 1
    for (; j < 40; j += 4) {
      tile(I0{}, I1{}, I1{}, j); tile(I1{}, I1{}, I1{}, j + 1); tile(I2{}, I1{}, I1{}, j + 2); tile(I3{}, I1{}, I1{}, j + 3);
    }
    tile(I0{}, I1{}, I1{}, 40); tile(I1{}, I1{}, I1{}, 41); tile(I2{}, I1{}, I1{}, 42); tile(I3{}, I1{}, I2{}, 43);
    asm volatile("s_nop 7\n\ts_nop 7\n\ts_nop 7" ::: "memory");
    SCHED();
  }
  STAMP(ws, 10);
  const int tile_id = rt * 8 + ct;
  char* slab_me = ws + WS_SLAB + (size_t)(tile_id * 2 + hs) * 32768;
  char* slab_pt = ws + WS_SLAB + (size_t)(tile_id * 2 + (hs ^ 1)) * 32768;
  GAS unsigned* flag_me = (GAS unsigned*)(ws + WS_FLAG) + (tile_id * 2 + hs) * 64;
  GAS unsigned* flag_pt = (GAS unsigned*)(ws + WS_FLAG) + (tile_id * 2 + (hs ^ 1)) * 64;
  asm volatile("s_waitcnt vmcnt(0) lgkmcnt(0)" ::: "memory");
  BAR();
  if (wid < 4) {
    if ((wid >> 1) != hs) {
      __amdgpu_buffer_rsrc_t rs = __builtin_amdgcn_make_buffer_rsrc(slab_me, 0, 32768, 0x00020000);
#pragma unroll
      for (int n = 0; n < 4; ++n)
#pragma unroll
        for (int i4 = 0; i4 < 4; ++i4) {
          const f32x4 v = {acc[n][4 * i4], acc[n][4 * i4 + 1], acc[n][4 * i4 + 2], acc[n][4 * i4 + 3]};
          __builtin_amdgcn_raw_buffer_store_b128(__builtin_bit_cast(u32x4, v), rs,
                                                 ((((wid & 1) * 4 + n) * 4 + i4) * 64 + lane) * 16, 0, 16);
        }
      asm volatile("s_waitcnt vmcnt(0)" ::: "memory");
    } else {
#pragma unroll
      for (int n = 0; n < 4; ++n)
#pragma unroll
        for (int i4 = 0; i4 < 4; ++i4) {
          const f32x4 v = {acc[n][4 * i4], acc[n][4 * i4 + 1], acc[n][4 * i4 + 2], acc[n][4 * i4 + 3]};
          *(LAS f32x4*)(lds + ((((wid & 1) * 4 + n) * 4 + i4) * 64 + lane) * 16) = v;
        }
      asm volatile("s_waitcnt lgkmcnt(0)" ::: "memory");
    }
  }
  const int t = rt * 128 + hs * 64 + (wid >> 2) * 32 + (lane & 31);
  const int d0 = ct * 128 + (wid & 3) * 32 + 4 * (lane >> 5);
  const float g = ((const float*)(ws + WS_G))[t];
  u32x2 xv[4], dv[4];
#pragma unroll
  for (int i4 = 0; i4 < 4; ++i4) {
    xv[i4] = *(const u32x2*)(x + (size_t)t * D_ + d0 + 8 * i4);
    dv[i4] = *(const u32x2*)(dskip + d0 + 8 * i4);
  }
  asm volatile("" ::: "memory");
  __builtin_amdgcn_s_barrier();
  asm volatile("" ::: "memory");
  STAMP(ws, 11);
  if (tid == 0) __hip_atomic_store(flag_me, 1u, RLX_AGENT);
  if (wid == 4) {
    unsigned spins = 0;
    while ((unsigned)__builtin_amdgcn_readfirstlane(__hip_atomic_load(flag_pt, RLX_AGENT)) != 1u) {
      if (++spins > (1u << 24)) break;
      __builtin_amdgcn_s_sleep(1);
    }
  }
  asm volatile("" ::: "memory");
  __builtin_amdgcn_s_barrier();
  asm volatile("" ::: "memory");
  STAMP(ws, 12);
  {
    __amdgpu_buffer_rsrc_t rs = __builtin_amdgcn_make_buffer_rsrc(slab_pt, 0, 32768, 0x00020000);
    u32x4 pv[4];
#pragma unroll
    for (int i4 = 0; i4 < 4; ++i4)
      pv[i4] = __builtin_amdgcn_raw_buffer_load_b128(rs, ((wid * 4 + i4) * 64 + lane) * 16, 0, 16);
#pragma unroll
    for (int i4 = 0; i4 < 4; ++i4) {
      f32x4 o = *(LAS f32x4*)(lds + ((wid * 4 + i4) * 64 + lane) * 16) + __builtin_bit_cast(f32x4, pv[i4]);
      o[0] += g * rbf(bf2f(xv[i4][0] & 0xffffu) * bf2f(dv[i4][0] & 0xffffu));
      o[1] += g * rbf(bf2f(xv[i4][0] >> 16) * bf2f(dv[i4][0] >> 16));
      o[2] += g * rbf(bf2f(xv[i4][1] & 0xffffu) * bf2f(dv[i4][1] & 0xffffu));
      o[3] += g * rbf(bf2f(xv[i4][1] >> 16) * bf2f(dv[i4][1] >> 16));
      *(f32x4*)(out + (size_t)t * D_ + d0 + 8 * i4) = o;
    }
  }
  STAMP(ws, 13);
}

__global__ __launch_bounds__(512, 2) void k_main(const u16* __restrict__ x, const u16* __restrict__ w1,
                                                 const u16* __restrict__ w3, const u16* __restrict__ bre,
                                                 const u16* __restrict__ bim, char* __restrict__ ws,
                                                 const u16* __restrict__ cre, const u16* __restrict__ cim,
                                                 const u16* __restrict__ w2, const u16* __restrict__ dskip,
                                                 float* __restrict__ out) {
  extern __shared__ __attribute__((aligned(16))) char shm[];
  phase1((LAS char*)shm, x, w1, w3, bre, bim, ws, cre, cim, w2);
  phase2((LAS char*)shm, x, cre, cim, w2, dskip, ws, out);
}

extern "C" void kernel_launch(void* const* d_in, const int* in_sizes, int n_in, void* d_out, int out_size,
                              void* d_ws, size_t ws_size, hipStream_t stream) {
  const u16* x = (const u16*)d_in[0];
  const u16* lre = (const u16*)d_in[1];
  const u16* lim = (const u16*)d_in[2];
  const u16* bre = (const u16*)d_in[3];
  const u16* bim = (const u16*)d_in[4];
  const u16* cre = (const u16*)d_in[5];
  const u16* cim = (const u16*)d_in[6];
  const u16* ldt = (const u16*)d_in[7];
  const u16* dsk = (const u16*)d_in[8];
  const u16* w1 = (const u16*)d_in[10];
  const u16* w3 = (const u16*)d_in[11];
  const u16* w2 = (const u16*)d_in[12];
  const u16* gw = (const u16*)d_in[13];
  char* ws = (char*)d_ws;
  k_prep<<<256, 512, 0, stream>>>(x, gw, lre, lim, ldt, ws, w1, w3, bre, bim, cre, cim, w2);
  (void)hipFuncSetAttribute((const void*)k_main, hipFuncAttributeMaxDynamicSharedMemorySize, G1_LDS);
  k_main<<<256, 512, G1_LDS, stream>>>(x, w1, w3, bre, bim, ws, cre, cim, w2, dsk, (float*)d_out);
}
```

```cpp
#include <hip/hip_runtime.h>
#include <hip/hip_bf16.h>
#include <stdint.h>
#include <type_traits>

typedef unsigned short u16;
typedef unsigned long long u64;
typedef short bf16x8 __attribute__((ext_vector_type(8)));
typedef short s16x4 __attribute__((ext_vector_type(4)));
typedef float f32x4 __attribute__((ext_vector_type(4)));
typedef float f32x2 __attribute__((ext_vector_type(2)));
typedef unsigned u32x4 __attribute__((ext_vector_type(4)));
typedef unsigned u32x2 __attribute__((ext_vector_type(2)));
#define LAS __attribute__((address_space(3)))
#define GAS __attribute__((address_space(1)))
#define RLX_AGENT __ATOMIC_RELAXED, __HIP_MEMORY_SCOPE_AGENT

#define PFD 6
#define W2S 26
constexpr int S_ = 2048, D_ = 1024, P_ = 1024, F_ = 3584, KT_ = 2 * P_ + F_;

constexpr size_t WS_GRAN = 0;
constexpr size_t WS_FLAG = 131072;
constexpr size_t WS_CNT  = WS_FLAG + 65536;
constexpr size_t WS_G    = WS_CNT + 2048;
constexpr size_t WS_TAB  = WS_G + 8192;
constexpr size_t WS_A2   = WS_TAB + 16384;
constexpr size_t WS_SLAB = WS_A2 + (size_t)S_ * KT_ * 2;

__device__ __forceinline__ float bf2f(unsigned v16) { return __uint_as_float(v16 << 16); }
__device__ __forceinline__ unsigned cvt_pk_bf16(float lo, float hi) {
  unsigned r; asm("v_cvt_pk_bf16_f32 %0, %1, %2" : "=v"(r) : "v"(lo), "v"(hi)); return r;
}
__device__ __forceinline__ float rbf(float f) { return __uint_as_float(cvt_pk_bf16(f, f) << 16); }

__global__ __launch_bounds__(512) void k_prep(const u16* __restrict__ x, const u16* __restrict__ gw,
                                              const u16* __restrict__ lre, const u16* __restrict__ lim,
                                              const u16* __restrict__ ldt, char* __restrict__ ws,
                                              const u16* __restrict__ w1, const u16* __restrict__ w3,
                                              const u16* __restrict__ bre, const u16* __restrict__ bim,
                                              const u16* __restrict__ cre, const u16* __restrict__ cim,
                                              const u16* __restrict__ w2) {
  const int tid = threadIdx.x, wid = tid >> 6, lane = tid & 63, b = blockIdx.x;
  const int pch = b * 4 + (tid & 3);
  const unsigned in_lr = lre[pch], in_li = lim[pch], in_dt = ldt[pch];
  u32x4 wacc = {0u, 0u, 0u, 0u};
  {
    const int xg = b & 7, j32 = b >> 3;
    constexpr int CPR = PFD * 4;
    constexpr int NX = (64 * CPR + 511) / 512, NW = (PFD * 2 * 56 + 511) / 512;
    u32x4 v[NX + NW + 1];
#pragma unroll
    for (int i = 0; i < NX; ++i) {
      int c = tid + i * 512; c = c < 64 * CPR ? c : 0;
      v[i] = *(const u32x4*)(x + (size_t)(64 * j32 + c / CPR) * D_ + (c % CPR) * 8);
    }
#pragma unroll
    for (int i = 0; i < NW; ++i) {
      int c = tid + i * 512; c = c < PFD * 2 * 56 ? c : 0;
      const int mat = c / (PFD * 56), r = c % (PFD * 56);
      v[NX + i] = *(const u32x4*)((mat ? w3 : w1) + (size_t)(j32 * PFD + r / 56) * F_ + xg * 448 + (r % 56) * 8);
    }
    {
      const int c = tid < 8 * CPR ? tid : 0, r = c / CPR;
      v[NX + NW] = *(const u32x4*)((r < 4 ? bre : bim) + (size_t)(128 * xg + 4 * j32 + (r & 3)) * D_ + (c % CPR) * 8);
    }
#pragma unroll
    for (int i = 0; i < NX + NW + 1; ++i) wacc ^= v[i];
  }
  {
    unsigned long long pc; unsigned len;
    asm volatile("s_getpc_b64 %0\ngm_prep_here:\n\ts_mov_b32 %1, gm_kmain_end-gm_prep_here" : "=s"(pc), "=s"(len));
    const unsigned o = (unsigned)((b >> 3) * 16 + (tid & 15)) * 128u;
    if (tid < 16 && o < len && len < 0x20000u) wacc[0] ^= *(const unsigned*)(pc + o);
  }
  if (tid < 64) ((u64*)(ws + WS_GRAN))[b * 64 + tid] = 0ull;
  if (tid == 64) ((unsigned*)(ws + WS_FLAG))[b * 64] = 0u;
  if (tid == 65 && b < 16) ((unsigned*)(ws + WS_CNT))[b * 32] = 0u;
  const int row = b * 8 + wid;
  const u32x4* xp = (const u32x4*)(x + (size_t)row * D_);
  const u32x4* gp = (const u32x4*)gw;
  float s = 0.f;
#pragma unroll
  for (int i = 0; i < 2; ++i) {
    u32x4 a = xp[lane + 64 * i], c = gp[lane + 64 * i];
#pragma unroll
    for (int j = 0; j < 4; ++j) {
      s += rbf(bf2f(a[j] & 0xffffu) * bf2f(c[j] & 0xffffu));
      s += rbf(bf2f(a[j] >> 16) * bf2f(c[j] >> 16));
    }
  }
#pragma unroll
  for (int o = 32; o >= 1; o >>= 1) s += __shfl_xor(s, o);
  if (lane == 0) {
    s = rbf(s);
    float g = rbf(1.0f / (1.0f + expf(-s)));
    ((float*)(ws + WS_G))[row] = g;
  }
  asm volatile("" :: "v"(wacc));
  if (tid >= 448 && tid < 452) {
    const int p = pch;
    const float lr = bf2f(in_lr), li = bf2f(in_li);
    const float dt = rbf(expf(bf2f(in_dt)));
    const float zr = dt * lr, zi = dt * li;
    const float e = expf(zr);
    float sn, cs; sincosf(zi, &sn, &cs);
    const float br = e * cs, bi = e * sn;
    const float nr = br - 1.0f, ni = bi;
    const float den = lr * lr + li * li;
    const float cr = (nr * lr + ni * li) / den, ci = (ni * lr - nr * li) / den;
    ((f32x4*)(ws + WS_TAB))[p] = (f32x4){br, bi, cr, ci};
  }
}

constexpr int G1_A = 0;
constexpr int G1_B = 65536;
constexpr int G1_W = 81920;
constexpr int G1_RING = 147456;
constexpr int G1_LDS = G1_RING + 8192;

typedef __amdgpu_buffer_rsrc_t rsrc_t;
__device__ __forceinline__ rsrc_t mk_rsrc(const void* p, unsigned bytes) {
  return __builtin_amdgcn_make_buffer_rsrc((void*)p, 0, bytes, 0x00020000);
}
__device__ __forceinline__ void dma16(rsrc_t rs, LAS char* dst, unsigned voff, unsigned soff) {
  asm volatile("s_mov_b32 m0, %0\n\ts_nop 0\n\tbuffer_load_dwordx4 %1, %2, %3 offen lds"
               :: "s"((unsigned)(size_t)dst), "v"(voff), "s"(rs), "s"(soff) : "memory");
}
__device__ __forceinline__ void touchg(const char* p, LAS char* dst) {
  asm volatile("s_mov_b32 m0, %0\n\ts_nop 0\n\tglobal_load_lds_dword %1, off" :: "s"((unsigned)(size_t)dst), "v"(p) : "memory");
}
__device__ __forceinline__ bf16x8 lds_b128(LAS char* p) { return *(LAS bf16x8*)p; }
__device__ __forceinline__ bf16x8 lds_tr2(LAS char* p0, LAS char* p1) {
  s16x4 a = __builtin_amdgcn_ds_read_tr16_b64_v4i16((LAS s16x4*)p0);
  s16x4 b = __builtin_amdgcn_ds_read_tr16_b64_v4i16((LAS s16x4*)p1);
  return __builtin_shufflevector(a, b, 0, 1, 2, 3, 4, 5, 6, 7);
}
#define MFMA_IP(c, a, b) asm volatile("v_mfma_f32_16x16x32_bf16 %0, %1, %2, %0" : "+v"(c) : "v"(a), "v"(b))

#define A2_STORE(p, v) asm volatile("global_store_dwordx4 %0, %1, off sc1\n\ts_nop 1" :: "v"(p), "v"(v) : "memory")
__device__ __forceinline__ u16* a2_chunk(u16* A2, int row, int kc) {
  return A2 + ((size_t)((row >> 5) * (KT_ / 16) + (kc >> 1)) * 64 + (kc & 1) * 32 + (row & 31)) * 8;
}
typedef float f32x16 __attribute__((ext_vector_type(16)));
#define MFMA32_IP(c, a, b) asm volatile("v_mfma_f32_32x32x16_bf16 %0, %1, %2, %0" : "+v"(c) : "v"(a), "v"(b))
#define MFMA32_IPS(c, a, b) asm volatile("s_nop 1\n\tv_mfma_f32_32x32x16_bf16 %0, %1, %2, %0" : "+v"(c) : "v"(a), "v"(b))
template <int CTRL> __device__ __forceinline__ float dppf(float v) {
  return __builtin_bit_cast(float, __builtin_amdgcn_update_dpp(0, __builtin_bit_cast(int, v), CTRL, 0xf, 0xf, true));
}
__device__ __forceinline__ float row16_sum(float v) {
  v += dppf<0xB1>(v);
  v += dppf<0x4E>(v);
  v += dppf<0x141>(v);
  v += dppf<0x140>(v);
  return v;
}
#define SCHED() __builtin_amdgcn_sched_barrier(0)
#define BAR() do { SCHED(); __builtin_amdgcn_s_barrier(); SCHED(); } while (0)
#define MFENCE() do { SCHED(); asm volatile("s_nop 7\n\ts_nop 7\n\ts_nop 7" ::: "memory"); SCHED(); } while (0)

struct G1Stage {
  rsrc_t rx, rw, r4;
  unsigned voE, voO;
  unsigned voW, voW2, vo4, vo5;
  unsigned so4, ld4;
  unsigned bs4;
  const char* pf0; const char* pf1; unsigned pfs1;
  const char *c0, *c1, *c2;
};

template <int ROLE>
__device__ __forceinline__ void gemm1_body(LAS char* lds, G1Stage& st, int wid, int wm, int lane, f32x4 (&acc)[4][9]) {
  constexpr int WN = ROLE == 0 ? 0 : 1;
  constexpr int NG = WN == 0 ? 4 : 3;
  constexpr int S0 = WN == 0 ? 0 : 1;
  constexpr int NA = ROLE == 2 ? 4 : 6, NW = ROLE == 2 ? 4 : 2, HA = NA / 2;
  const int fr = lane & 15, fq = lane >> 4;
  const int frag = fr * 128 + ((fq ^ (fr >> 1)) << 4);
  const int a_off = G1_A + wm * 8192 + frag;
  const int s_off = G1_B + S0 * 2048 + frag;
  const int q = (lane & 15) >> 2, p = lane & 3;
  const int w_base = G1_W + WN * 4096 + (8 * fq + q) * 128 + ((((q >> 1) & 1) | ((fq & 1) << 1)) << 5) + p * 8;

  auto apiece = [&](int k, int pr) {
    if (ROLE < 3 && k < NA) {
      const unsigned vo = (k & 1) ? st.voO : st.voE;
      if (k < 4) {
        const int ap = 14 * (k >> 1) + 2 * wid + (k & 1);
        dma16(st.rx, lds + G1_A + (pr & 1) * 32768 + ap * 1024, vo, pr * 128 + ap * 16384);
      } else {
        dma16(st.r4, lds + st.ld4 + (pr & 1) * st.bs4 + (k & 1) * 1024, vo, pr * 128 + st.so4 + (k & 1) * 16384);
      }
    }
  };
  auto wpiece = [&](int k, int j) {
    if (ROLE < 3 && k < NW) {
      LAS char* d = lds + G1_W + (j & 3) * 16384;
      if (k == 0) dma16(st.rw, d + (wid >> 2) * 8192 + (wid & 3) * 1024, st.voW, j * (32 * F_ * 2));
      if (k == 1) dma16(st.rw, d + (wid >> 2) * 8192 + (wid & 3) * 1024 + 4096, st.voW2, j * (32 * F_ * 2));
      if (k == 2) dma16(st.rw, d + 8192 + 3 * 1024, st.vo4, j * (32 * F_ * 2));
      if (k == 3) dma16(st.rw, d + 8192 + 3 * 1024 + 4096, st.vo5, j * (32 * F_ * 2));
    }
  };
  auto step = [&](auto J4, auto TAILT, int j) {
    constexpr int j4 = decltype(J4)::value, TAIL = decltype(TAILT)::value;
    constexpr bool odd = j4 & 1, issue = TAIL == 0;
    const int pr = j >> 1;
    LAS char* ab = lds + ((j4 >> 1) & 1) * 32768;
    LAS char* bb = lds + ((j4 >> 1) & 1) * 8192;
    LAS char* wb = lds + j4 * 16384;
    bf16x8 xa[4], fb[9];
    if (ROLE == 0) {
      if (issue && !odd) apiece(0, pr + 1);
      if (odd && TAIL <= 29) wpiece(0, j + 2);
    } else if (issue && !odd) apiece(HA, pr + 1);
#pragma unroll
    for (int m = 0; m < 4; ++m) xa[m] = lds_b128(ab + ((a_off + m * 2048) ^ (odd ? 64 : 0)));
#pragma unroll
    for (int i = 0; i < NG; ++i) {
      fb[i] = lds_tr2(wb + (w_base ^ (i << 5)), wb + (w_base ^ (i << 5)) + 512);
      fb[4 + i] = lds_tr2(wb + (w_base ^ (i << 5)) + 8192, wb + (w_base ^ (i << 5)) + 512 + 8192);
    }
    if (WN == 0) fb[8] = lds_b128(bb + (s_off ^ (odd ? 64 : 0)));
    else {
      fb[3] = lds_b128(bb + (s_off ^ (odd ? 64 : 0)));
      fb[7] = lds_b128(bb + ((s_off + 2048) ^ (odd ? 64 : 0)));
      fb[8] = lds_b128(bb + ((s_off + 4096) ^ (odd ? 64 : 0)));
    }
    if (ROLE == 3) {
      if (j + PFD < 32) {
        if (!odd) { touchg(st.pf0, lds + G1_RING + 4096); st.pf0 += 128; }
        else { touchg(st.pf1, lds + G1_RING + 4096 + 256); st.pf1 += st.pfs1; }
      }
      if (j >= W2S && j < W2S + 6) {
        const int jj = j - W2S;
        const char* pb = jj == 0 ? st.c0 : (jj == 1 ? st.c1 : st.c2 + (jj - 2) * 8192);
        touchg(pb + ((jj == 5 ? lane & 31 : lane) * 128), lds + G1_RING + 4096 + 512);
      }
      asm volatile("s_waitcnt lgkmcnt(0)" ::: "memory");
    } else if (ROLE == 0) {
      if (issue && !odd) { apiece(1, pr + 1); apiece(2, pr + 1); }
      if (odd && TAIL <= 29) wpiece(1, j + 2);
      asm volatile("s_waitcnt lgkmcnt(0)" ::: "memory");
    } else {
      if (issue && !odd) {
#pragma unroll
        for (int k = HA + 1; k < NA; ++k) apiece(k, pr + 1);
      }
      if (odd && TAIL <= 29) {
#pragma unroll
        for (int k = 0; k < NW; ++k) wpiece(k, j + 2);
      }
      if (odd && issue) {
#pragma unroll
        for (int k = 0; k < NW; ++k) wpiece(k, j + 3);
      }
      constexpr int vm = TAIL == 0 ? (odd ? 2 * NW : NW + NA) : (TAIL == 29 ? NW : 0);
      asm volatile("s_waitcnt vmcnt(%0) lgkmcnt(0)" :: "n"(vm) : "memory");
    }
    BAR();
    __builtin_amdgcn_s_setprio(1);
#pragma unroll
    for (int i = 0; i < 9; ++i) {
#pragma unroll
      for (int m = 0; m < 4; ++m) MFMA_IP(acc[m][i], fb[i], xa[m]);
      if (issue) {
        if (ROLE == 0) {
          if (!odd) {
            if (i == 1) apiece(3, pr + 1);
            if (i == 3) apiece(4, pr + 1);
            if (i == 5) apiece(5, pr + 1);
          } else {
            if (i == 2) wpiece(0, j + 3);
            if (i == 5) wpiece(1, j + 3);
          }
        } else if (ROLE < 3 && odd && j < 29) {
          if (i == 1) apiece(0, pr + 2);
          if (i == 3) apiece(1, pr + 2);
          if (i == 5 && HA > 2) apiece(2, pr + 2);
        }
      }
    }
    __builtin_amdgcn_s_setprio(0);
    if (ROLE == 0) {
      constexpr int vm = TAIL == 0 ? (odd ? 4 : 8) : (TAIL == 29 ? 2 : 0);
      asm volatile("s_waitcnt vmcnt(%0)" :: "n"(vm) : "memory");
    }
    BAR();
  };
  using I0 = std::integral_constant<int, 0>; using I1 = std::integral_constant<int, 1>;
  using I2 = std::integral_constant<int, 2>; using I3 = std::integral_constant<int, 3>;

#pragma unroll
  for (int k = 0; k < 6; ++k) apiece(k, 0);
#pragma unroll
  for (int j = 0; j < 3; ++j)
#pragma unroll
    for (int k = 0; k < 4; ++k) wpiece(k, j);
  if (ROLE == 1 || ROLE == 2) {
#pragma unroll
    for (int k = 0; k < HA; ++k) apiece(k, 1);
  }
  if (ROLE == 3) {
    dma16(st.rx, lds + G1_RING + 1024, (unsigned)lane * 16u, 0u);
    dma16(st.r4, lds + G1_RING + 2048, (unsigned)lane * 16u, 0u);
  } else {
    asm volatile("s_waitcnt vmcnt(%0)" :: "n"(ROLE == 0 ? 2 * NW : 2 * NW + HA) : "memory");
  }
  BAR();
  if (WN == 1) BAR();
#pragma unroll 1
  for (int j = 0; j < 28; j += 4) {
    step(I0{}, I0{}, j); step(I1{}, I0{}, j + 1); step(I2{}, I0{}, j + 2); step(I3{}, I0{}, j + 3);
  }
  step(I0{}, I0{}, 28);
  step(I1{}, std::integral_constant<int, 29>{}, 29);
  step(I2{}, std::integral_constant<int, 30>{}, 30);
  step(I3{}, std::integral_constant<int, 31>{}, 31);
  if (WN == 0) BAR();
  if (ROLE == 3) asm volatile("s_waitcnt vmcnt(11)\n\ts_nop 7\n\ts_nop 7" ::: "memory");
  else asm volatile("s_waitcnt vmcnt(0)\n\ts_nop 7\n\ts_nop 7" ::: "memory");
  SCHED();
}

__device__ __forceinline__ int glu_blk(int l0, int c4, int nb) {
  const int full = 8 * c4;
  const int lo = 7 * c4;
  const int r = nb - 4, pre = full - lo;
  const int b = nb < 4 ? full + nb : (r < pre ? lo + r : full + 4 + (r - pre));
  return l0 * 4 + b;
}

__device__ __forceinline__ void p2_loader(LAS char* lds, const u16* __restrict__ cre,
                                          const u16* __restrict__ cim, const u16* __restrict__ w2, char* __restrict__ ws);
__device__ __forceinline__ void p2_compute(f32x16 (&acc)[4], LAS char* lds, char* __restrict__ ws);

__device__ __forceinline__ void phase1(f32x16 (&acc2)[4], LAS char* lds, const u16* __restrict__ x, const u16* __restrict__ w1,
                                       const u16* __restrict__ w3, const u16* __restrict__ bre,
                                       const u16* __restrict__ bim, char* __restrict__ ws,
                                       const u16* __restrict__ cre, const u16* __restrict__ cim,
                                       const u16* __restrict__ w2) {
  const int tid = threadIdx.x, lane = tid & 63;
  const int wid = __builtin_amdgcn_readfirstlane(tid >> 6);
  const int wm = wid & 3, wn = wid >> 2;
  const int bid = blockIdx.x;
  const int cb = (bid & 7) * 4 + ((bid >> 3) & 3), rb = bid >> 5;

  G1Stage st;
  {
    const int r = lane >> 3;
    st.voE = (unsigned)(r * D_ + (((lane & 7) ^ (r >> 1)) * 8)) * 2u;
    st.voO = (unsigned)(r * D_ + (((lane & 7) ^ (4 + (r >> 1))) * 8)) * 2u;
  }
  const int c4 = (bid >> 3) & 3, l0 = (bid & 7) * 7;
  {
    const int r = lane >> 3, pos = lane & 7;
    auto woff = [&](int kb, int half) -> unsigned {
      const int k = 8 * kb + r, f = ((k >> 1) & 1) | (((k >> 3) & 1) << 1), b = (pos >> 1) ^ f;
      const int blk = glu_blk(l0, c4, half * 4 + (b < 3 || half == 0 ? b : 0));
      return (half == 1 && b == 3) ? 0x80000000u : (unsigned)(k * F_ + blk * 16 + (pos & 1) * 8) * 2u;
    };
    st.voW = woff(wid & 3, 0); st.voW2 = woff(wid & 3, 1);
    st.vo4 = woff(3, 0); st.vo5 = woff(3, 1);
  }
  st.rx = mk_rsrc(x + (size_t)rb * 256 * D_, 256u * D_ * 2u);
  st.rw = mk_rsrc(wn ? w3 : w1, (unsigned)(D_ * F_) * 2u);
  if (wid < 2) {
    st.r4 = st.rx; st.so4 = (unsigned)(28 + 2 * wid) * 16384u; st.ld4 = G1_A + (28 + 2 * wid) * 1024; st.bs4 = 32768u;
  } else {
    const int bp = (2 * wid - 4) & 7;
    st.r4 = mk_rsrc((bp < 4 ? bre : bim) + (size_t)cb * 32 * D_, 32u * D_ * 2u);
    st.so4 = (unsigned)(bp & 3) * 16384u; st.ld4 = G1_B + bp * 1024; st.bs4 = 8192u;
  }
  if (wid == 7) {
    st.rx = mk_rsrc(ws + WS_G + rb * 1024, 1024u);
    st.r4 = mk_rsrc(ws + WS_TAB + cb * 512, 512u);
  }
  {
    const int xg = bid & 7, j32 = bid >> 3;
    st.pf0 = (const char*)(x + (size_t)(64 * j32 + lane) * D_) + PFD * 64;
    if (lane < 28) {
      const int l = lane % 14;
      st.pf1 = (const char*)((lane < 14 ? w1 : w3) + (size_t)(PFD * 32 + 2 * j32 + l / 7) * F_ + xg * 448) + (l % 7) * 128;
      st.pfs1 = 64u * F_ * 2u;
    } else {
      const int l = (lane - 28) & 7;
      st.pf1 = (const char*)((l < 4 ? bre : bim) + (size_t)(128 * xg + 4 * j32 + (l & 3)) * D_) + PFD * 64;
      st.pfs1 = 128u;
    }
    st.c0 = (const char*)cre + (size_t)bid * 8192;
    st.c1 = (const char*)cim + (size_t)bid * 8192;
    st.c2 = (const char*)w2 + (size_t)bid * 28672;
  }

  f32x4 acc[4][9];
#pragma unroll
  for (int m = 0; m < 4; ++m)
#pragma unroll
    for (int i = 0; i < 9; ++i) acc[m][i] = (f32x4){0, 0, 0, 0};
  if (wid < 4)       gemm1_body<0>(lds, st, wid, wm, lane, acc);
  else if (wid < 6)  gemm1_body<1>(lds, st, wid, wm, lane, acc);
  else if (wid == 6) gemm1_body<2>(lds, st, wid, wm, lane, acc);
  else               gemm1_body<3>(lds, st, wid, wm, lane, acc);

  const int fr = lane & 15, fq = lane >> 4;
  LAS float* U = (LAS float*)lds;
#pragma unroll
  for (int m = 0; m < 4; ++m) {
    const int t = wm * 64 + m * 16 + fr;
    LAS float* u = U + t * 68 + (t >> 4) * 4 + 4 * fq;
    if (wn == 0) {
      *(LAS f32x4*)(u) = acc[m][8];
    } else {
      *(LAS f32x4*)(u + 16) = acc[m][3];
      *(LAS f32x4*)(u + 32) = acc[m][7];
      *(LAS f32x4*)(u + 48) = acc[m][8];
    }
  }
  const int p = wid * 4 + fq, seg = fr;
  asm volatile("s_waitcnt lgkmcnt(0)" ::: "memory");
  __builtin_amdgcn_s_barrier();
  asm volatile("" ::: "memory");
  const f32x4 tb = ((LAS f32x4*)(lds + G1_RING + 2048))[p];
  LAS float* gv = (LAS float*)(lds + G1_RING + 1024);
  const float lr = tb[0], li = tb[1];
  float sr[16], si[16];
#pragma unroll
  for (int i = 0; i < 16; ++i) {
    sr[i] = U[(seg * 16 + i) * 68 + seg * 4 + p];
    si[i] = U[(seg * 16 + i) * 68 + seg * 4 + 32 + p];
  }
#pragma unroll
  for (int i = 1; i < 16; ++i) {
    const float a = sr[i - 1], b = si[i - 1];
    sr[i] += lr * a - li * b;
    si[i] += lr * b + li * a;
  }
  float pr_ = lr, pi_ = li;
#pragma unroll
  for (int i = 0; i < 4; ++i) { const float a = pr_ * pr_ - pi_ * pi_, b = 2.f * pr_ * pi_; pr_ = a; pi_ = b; }
  float tr_ = sr[15], ti_ = si[15];
  float lsr = 1.f, lsi = 0.f;
  auto lvl = [&](auto DD) {
    constexpr int d = decltype(DD)::value;
    const float ar = dppf<0x110 + d>(tr_), ai = dppf<0x110 + d>(ti_);
    tr_ += pr_ * ar - pi_ * ai; ti_ += pr_ * ai + pi_ * ar;
    if (seg & d) { const float a = lsr * pr_ - lsi * pi_, b = lsr * pi_ + lsi * pr_; lsr = a; lsi = b; }
    const float a = pr_ * pr_ - pi_ * pi_, b = 2.f * pr_ * pi_; pr_ = a; pi_ = b;
  };
  lvl(std::integral_constant<int, 1>{}); lvl(std::integral_constant<int, 2>{});
  lvl(std::integral_constant<int, 4>{}); lvl(std::integral_constant<int, 8>{});
  GAS u64* gran = (GAS u64*)(ws + WS_GRAN) + (size_t)cb * 8 * 64;
  if (seg == 15 && rb < 7) {
    __hip_atomic_store(gran + rb * 64 + p, (1ull << 32) | (u64)__float_as_uint(tr_), RLX_AGENT);
    __hip_atomic_store(gran + rb * 64 + 32 + p, (1ull << 32) | (u64)__float_as_uint(ti_), RLX_AGENT);
  }
  float cr = dppf<0x111>(tr_), ci = dppf<0x111>(ti_);
  u16* A2 = (u16*)(ws + WS_A2);
  {
    LAS char* HT = lds + 73728;
    const int ng = wn == 0 ? 4 : 3;
#pragma unroll
    for (int m = 0; m < 4; ++m) {
      const int tl = wm * 64 + m * 16 + fr;
      const float omg = 1.0f - gv[tl];
#pragma unroll
      for (int i = 0; i < 4; ++i) {
        if (i < ng) {
          const f32x4 a = acc[m][i];
          const f32x4 au = a * acc[m][4 + i] * omg;
          const f32x4 z = a * -1.44269504089f;
          f32x4 e;
#pragma unroll
          for (int j = 0; j < 4; ++j) e[j] = __builtin_amdgcn_exp2f(z[j]);
          e = e + 1.0f;
          f32x4 r;
#pragma unroll
          for (int j = 0; j < 4; ++j) r[j] = __builtin_amdgcn_rcpf(e[j]);
          const f32x4 h = au * r;
          *(LAS u32x2*)(HT + tl * 240 + (wn * 4 + i) * 32 + fq * 8) = (u32x2){cvt_pk_bf16(h[0], h[1]), cvt_pk_bf16(h[2], h[3])};
        }
      }
    }
  }
  float hr = 0.f, hi = 0.f;
  if (seg < rb) {
    u64 v0, v1; unsigned spins = 0;
    for (;;) {
      v0 = __hip_atomic_load(gran + seg * 64 + p, RLX_AGENT);
      v1 = __hip_atomic_load(gran + seg * 64 + 32 + p, RLX_AGENT);
      if ((v0 >> 32) == 1ull && (v1 >> 32) == 1ull) break;
      if (++spins > (1u << 24)) break;
      __builtin_amdgcn_s_sleep(1);
    }
    hr = __uint_as_float((unsigned)v0); hi = __uint_as_float((unsigned)v1);
    for (int e = rb - 1 - seg; e > 0; --e) { const float a = pr_ * hr - pi_ * hi, b = pr_ * hi + pi_ * hr; hr = a; hi = b; }
  }
  hr = row16_sum(hr); hi = row16_sum(hi);
  cr += lsr * hr - lsi * hi;
  ci += lsr * hi + lsi * hr;
  asm volatile("s_waitcnt lgkmcnt(0)" ::: "memory");
  __builtin_amdgcn_s_barrier();
  asm volatile("" ::: "memory");
  {
    LAS char* HT = lds + 73728;
#pragma unroll
    for (int i = 0; i < 7; ++i) {
      const int idx = tid + 512 * i, u = idx >> 5, c = u % 14, t = (u / 14) * 32 + (idx & 31), nb = c >> 1;
      const int blk = glu_blk(l0, c4, nb);
      const u32x4 v = *(LAS u32x4*)(HT + t * 240 + c * 16);
      A2_STORE(a2_chunk(A2, rb * 256 + t, 256 + blk * 2 + (c & 1)), v);
    }
  }
  LAS char* HS = lds;
  const float cfr = tb[2], cfi = tb[3];
#pragma unroll
  for (int i = 0; i < 16; ++i) {
    const float nr = lr * cr - li * ci, ni = lr * ci + li * cr;
    cr = nr; ci = ni;
    const float a = sr[i] + cr, b = si[i] + ci;
    const float g2 = 2.0f * gv[seg * 16 + i];
    const float orr = (cfr * a - cfi * b) * g2, oi = -(cfr * b + cfi * a) * g2;
    const unsigned pk = cvt_pk_bf16(orr, oi);
    LAS u16* hp = (LAS u16*)(HS + (seg * 16 + i) * 128 + seg * 16) + p;
    hp[0] = (u16)(pk & 0xffffu);
    hp[32] = (u16)(pk >> 16);
  }
  asm volatile("s_waitcnt vmcnt(0) lgkmcnt(0)" ::: "memory");
  __builtin_amdgcn_s_barrier();
  asm volatile("" ::: "memory");
  if (tid == 0) __hip_atomic_fetch_add((GAS unsigned*)(ws + WS_CNT) + rb * 64, 1u, RLX_AGENT);
#pragma unroll
  for (int i = 0; i < 4; ++i) {
    const int idx = tid + 512 * i, c = (idx >> 5) & 7, t = (idx >> 8) * 32 + (idx & 31);
    const u32x4 v = *(LAS u32x4*)(HS + t * 128 + c * 16 + (t >> 4) * 16);
    A2_STORE(a2_chunk(A2, rb * 256 + t, (c >> 2) * 128 + cb * 4 + (c & 3)), v);
  }
  asm volatile("s_waitcnt lgkmcnt(0)" ::: "memory");
  __builtin_amdgcn_s_barrier();
  asm volatile("" ::: "memory");
#pragma unroll
  for (int n = 0; n < 4; ++n)
#pragma unroll
    for (int i = 0; i < 16; ++i) acc2[n][i] = 0.f;
  if (wn == 1) p2_loader(lds, cre, cim, w2, ws);
  else p2_compute(acc2, lds, ws);
}

constexpr int G2_STAGE = 16384, G2_NBUF = 8, G2_LDS = G2_STAGE * G2_NBUF;
constexpr int G2_NKT = 44;

__device__ __forceinline__ void p2_loader(LAS char* lds, const u16* __restrict__ cre,
                                          const u16* __restrict__ cim, const u16* __restrict__ w2, char* __restrict__ ws) {
  const int tid = threadIdx.x, lane = tid & 63;
  const int wid = __builtin_amdgcn_readfirstlane(tid >> 6);
  const int bid = blockIdx.x, xcd = bid & 7, idx = bid >> 3;
  const int hs = xcd >> 2, rt = (xcd & 3) * 4 + (idx & 3), ct = idx >> 2;
  auto ktile = [&](int j) { return j < 28 ? 32 + 28 * hs + j : 16 * hs + (j - 28); };
  (void)rt;
  {
    const int l = wid - 4;
    const int arow = 8 * l + (lane >> 3);
    const unsigned voC = (unsigned)(arow * P_ + ((lane & 7) ^ ((arow >> 1) & 7)) * 8) * 2u;
    const unsigned voW = (unsigned)(arow * D_ + ((((lane & 7) >> 1) ^ (arow & 2)) * 16) + (lane & 1) * 8) * 2u;
    const rsrc_t rc = mk_rsrc((hs ? cim : cre) + (size_t)ct * 128 * P_, 128u * P_ * 2u);
    const rsrc_t rw = mk_rsrc(w2 + (size_t)hs * 28 * 64 * D_ + ct * 128, (unsigned)(28 * 64 * D_ - ct * 128) * 2u);
    auto stageW = [&](int j) {
      LAS char* d = lds + (j & 7) * G2_STAGE + l * 1024;
#pragma unroll
      for (int i = 0; i < 4; ++i)
        dma16(rw, d + i * 4096, voW, j * (64 * D_ * 2) + (i & 1) * (32 * D_ * 2) + (i >> 1) * 128);
    };
    auto stageC = [&](int j) {
      LAS char* d = lds + (j & 7) * G2_STAGE + l * 1024;
#pragma unroll
      for (int i = 0; i < 4; ++i) dma16(rc, d + i * 4096, voC, (j - 28) * 128 + i * (32 * P_ * 2));
    };
#pragma unroll
    for (int t = 0; t < 8; ++t) stageW(t);
    asm volatile("s_waitcnt vmcnt(24)" ::: "memory");
    BAR();
#pragma unroll 1
    for (int m = 1; m <= 10; ++m) {
      asm volatile("s_waitcnt vmcnt(16)" ::: "memory");
      BAR();
      stageW(2 * m + 6); stageW(2 * m + 7);
    }
#pragma unroll 1
    for (int m = 11; m <= 18; ++m) {
      asm volatile("s_waitcnt vmcnt(16)" ::: "memory");
      BAR();
      stageC(2 * m + 6); stageC(2 * m + 7);
    }
    asm volatile("s_waitcnt vmcnt(16)" ::: "memory"); BAR();
    asm volatile("s_waitcnt vmcnt(8)" ::: "memory"); BAR();
    asm volatile("s_waitcnt vmcnt(0)" ::: "memory"); BAR();
  }
}

__device__ __forceinline__ void p2_compute(f32x16 (&acc)[4], LAS char* lds, char* __restrict__ ws) {
  const int tid = threadIdx.x, lane = tid & 63;
  const int wid = __builtin_amdgcn_readfirstlane(tid >> 6);
  const int bid = blockIdx.x, xcd = bid & 7, idx = bid >> 3;
  const int hs = xcd >> 2, rt = (xcd & 3) * 4 + (idx & 3), ct = idx >> 2;
  auto ktile = [&](int j) { return j < 28 ? 32 + 28 * hs + j : 16 * hs + (j - 28); };
  (void)ct;
  {
    const int r32 = lane & 31, h = lane >> 5;
    const int c_off = r32 * 128 + ((h ^ ((r32 >> 1) & 7)) << 4);
    const int q = (lane & 15) >> 2, p = lane & 3;
    const int w_base = (8 * h + q) * 128 + ((((lane >> 4) & 1) ^ (q & 2)) << 5) + p * 8;
    const rsrc_t ra = mk_rsrc(ws + WS_A2 + (size_t)(rt * 4 + wid) * (KT_ / 16) * 1024, (unsigned)(KT_ / 16) * 1024u);
    const unsigned voA = (unsigned)lane * 16u;
    bf16x8 fa[4][4];
    bf16x8 fb[2][4];
    auto lda1 = [&](auto SL, int j, int s2) {
      constexpr int sl = decltype(SL)::value;
      const int jj = j < G2_NKT ? j : G2_NKT - 1;
      fa[sl][s2] = __builtin_bit_cast(bf16x8, __builtin_amdgcn_raw_buffer_load_b128(ra, voA, (ktile(jj) * 4 + s2) * 1024, 16));
    };
    auto rd = [&](auto TYPE, int slot, int s2, int n) {
      constexpr int ty = decltype(TYPE)::value;
      LAS char* base = lds + slot * G2_STAGE;
      if (ty == 0) fb[s2 & 1][n] = lds_b128(base + ((c_off + n * 4096) ^ (s2 << 5)));
      else {
        LAS char* pp = base + ((w_base + (n >> 1) * 8192) ^ ((n & 1) << 6)) + s2 * 2048;
        fb[s2 & 1][n] = lds_tr2(pp, pp + 512);
      }
    };
    using I0 = std::integral_constant<int, 0>; using I1 = std::integral_constant<int, 1>;
    using I2 = std::integral_constant<int, 2>; using I3 = std::integral_constant<int, 3>;
    auto tile = [&](auto SL, auto TY, auto NT, int j, bool safe = false) {
      constexpr int nt = decltype(NT)::value, sl = decltype(SL)::value;
#pragma unroll
      for (int s2 = 0; s2 < 4; ++s2) {
        if (s2 == 3 && (sl & 1)) {
          asm volatile("s_waitcnt lgkmcnt(0)" ::: "memory");
          if (nt < 2) BAR();
        }
#pragma unroll
        for (int n = 0; n < 4; ++n) {
          if (s2 < 3) rd(TY, j & 7, s2 + 1, n);
          else if (nt == 0) rd(I0{}, (j + 1) & 7, 0, n);
          else if (nt == 1) rd(I1{}, (j + 1) & 7, 0, n);
          if (safe) MFMA32_IPS(acc[n], fb[s2 & 1][n], fa[sl][s2]);
          else MFMA32_IP(acc[n], fb[s2 & 1][n], fa[sl][s2]);
          SCHED();
        }
        lda1(SL, j + 4, s2);
        SCHED();
      }
    };
    auto wait_cnt = [&](int part) {
      GAS unsigned* cnt = (GAS unsigned*)(ws + WS_CNT) + (rt >> 1) * 64 + part * 32;
      unsigned spins = 0;
      while ((unsigned)__builtin_amdgcn_readfirstlane(__hip_atomic_load(cnt, RLX_AGENT)) < 32u) {
        if (++spins > (1u << 24)) break;
        __builtin_amdgcn_s_sleep(1);
      }
      asm volatile("" ::: "memory");
    };
    wait_cnt(0);
    __builtin_amdgcn_s_setprio(3);
#pragma unroll
    for (int s2 = 0; s2 < 4; ++s2) { lda1(I0{}, 0, s2); SCHED(); }
#pragma unroll
    for (int s2 = 0; s2 < 4; ++s2) { lda1(I1{}, 1, s2); SCHED(); }
#pragma unroll
    for (int s2 = 0; s2 < 4; ++s2) { lda1(I2{}, 2, s2); SCHED(); }
#pragma unroll
    for (int s2 = 0; s2 < 4; ++s2) { lda1(I3{}, 3, s2); SCHED(); }
    asm volatile("s_waitcnt vmcnt(16)" ::: "memory");
    BAR();
    if (tid == 0) __hip_atomic_fetch_add((GAS unsigned*)(ws + WS_CNT) + (bid >> 5) * 64 + 32, 1u, RLX_AGENT);
#pragma unroll
    for (int n = 0; n < 4; ++n) rd(I1{}, 0, 0, n);
    MFENCE();
#pragma unroll 1
    for (int j = 0; j < 20; j += 4) {
      tile(I0{}, I1{}, I1{}, j); tile(I1{}, I1{}, I1{}, j + 1); tile(I2{}, I1{}, I1{}, j + 2); tile(I3{}, I1{}, I1{}, j + 3);
    }
    MFENCE();
    unsigned cv;
    asm volatile("global_load_dword %0, %1, off sc1" : "=v"(cv) : "v"((const unsigned*)(ws + WS_CNT) + (rt >> 1) * 64 + 32) : "memory");
    tile(I0{}, I1{}, I1{}, 20, true); tile(I1{}, I1{}, I1{}, 21); tile(I2{}, I1{}, I1{}, 22); tile(I3{}, I1{}, I1{}, 23);
    asm volatile("s_waitcnt vmcnt(16)" : "+v"(cv) :: "memory");
    if ((unsigned)__builtin_amdgcn_readfirstlane(cv) < 32u) { MFENCE(); wait_cnt(1); }
    MFENCE();
    tile(I0{}, I1{}, I1{}, 24, true); tile(I1{}, I1{}, I1{}, 25); tile(I2{}, I1{}, I1{}, 26); tile(I3{}, I1{}, I0{}, 27);
    MFENCE();
#pragma unroll 1
    for (int j = 28; j < 40; j += 4) {
      tile(I0{}, I0{}, I0{}, j); tile(I1{}, I0{}, I0{}, j + 1); tile(I2{}, I0{}, I0{}, j + 2); tile(I3{}, I0{}, I0{}, j + 3);
    }
    MFENCE();
    tile(I0{}, I0{}, I0{}, 40, true); tile(I1{}, I0{}, I0{}, 41); tile(I2{}, I0{}, I0{}, 42); tile(I3{}, I0{}, I2{}, 43);
    asm volatile("s_nop 7\n\ts_nop 7\n\ts_nop 7" ::: "memory");
    __builtin_amdgcn_s_setprio(0);
    SCHED();
    }
}

__device__ __forceinline__ void p2_finish(f32x16 (&acc)[4], LAS char* lds, const u16* __restrict__ x,
                                          const u16* __restrict__ dskip, char* __restrict__ ws, float* __restrict__ out) {
  const int tid = threadIdx.x, lane = tid & 63;
  const int wid = __builtin_amdgcn_readfirstlane(tid >> 6);
  const int bid = blockIdx.x, xcd = bid & 7, idx = bid >> 3;
  const int hs = xcd >> 2, rt = (xcd & 3) * 4 + (idx & 3), ct = idx >> 2;
  const int tile_id = rt * 8 + ct;
  char* slab_me = ws + WS_SLAB + (size_t)(tile_id * 2 + hs) * 32768;
  char* slab_pt = ws + WS_SLAB + (size_t)(tile_id * 2 + (hs ^ 1)) * 32768;
  GAS unsigned* flag_me = (GAS unsigned*)(ws + WS_FLAG) + (tile_id * 2 + hs) * 64;
  GAS unsigned* flag_pt = (GAS unsigned*)(ws + WS_FLAG) + (tile_id * 2 + (hs ^ 1)) * 64;
  asm volatile("s_waitcnt vmcnt(0) lgkmcnt(0)" ::: "memory");
  BAR();
  if (wid < 4) {
    __amdgpu_buffer_rsrc_t rs = __builtin_amdgcn_make_buffer_rsrc(slab_me, 0, 32768, 0x00020000);
#pragma unroll
    for (int nn = 0; nn < 2; ++nn)
#pragma unroll
      for (int i4 = 0; i4 < 4; ++i4) {
        const f32x16& a0 = acc[nn];
        const f32x16& a1 = acc[2 + nn];
        const f32x4 lo = {a0[4 * i4], a0[4 * i4 + 1], a0[4 * i4 + 2], a0[4 * i4 + 3]};
        const f32x4 hi = {a1[4 * i4], a1[4 * i4 + 1], a1[4 * i4 + 2], a1[4 * i4 + 3]};
        __builtin_amdgcn_raw_buffer_store_b128(__builtin_bit_cast(u32x4, hs ? lo : hi), rs,
                                               (((wid * 2 + nn) * 4 + i4) * 64 + lane) * 16, 0, 16);
        *(LAS f32x4*)(lds + (((wid * 2 + nn) * 4 + i4) * 64 + lane) * 16) = hs ? hi : lo;
      }
    asm volatile("s_waitcnt vmcnt(0) lgkmcnt(0)" ::: "memory");
  }
  const int t = rt * 128 + (wid >> 1) * 32 + (lane & 31);
  const int d0 = ct * 128 + hs * 64 + (wid & 1) * 32 + 4 * (lane >> 5);
  const float g = ((const float*)(ws + WS_G))[t];
  u32x2 xv[4], dv[4];
#pragma unroll
  for (int i4 = 0; i4 < 4; ++i4) {
    xv[i4] = *(const u32x2*)(x + (size_t)t * D_ + d0 + 8 * i4);
    dv[i4] = *(const u32x2*)(dskip + d0 + 8 * i4);
  }
  asm volatile("" ::: "memory");
  __builtin_amdgcn_s_barrier();
  asm volatile("" ::: "memory");
  if (tid == 0) __hip_atomic_store(flag_me, 1u, RLX_AGENT);
  if (wid == 4) {
    unsigned spins = 0;
    while ((unsigned)__builtin_amdgcn_readfirstlane(__hip_atomic_load(flag_pt, RLX_AGENT)) != 1u) {
      if (++spins > (1u << 24)) break;
      __builtin_amdgcn_s_sleep(1);
    }
  }
  asm volatile("" ::: "memory");
  __builtin_amdgcn_s_barrier();
  asm volatile("" ::: "memory");
  {
    __amdgpu_buffer_rsrc_t rs = __builtin_amdgcn_make_buffer_rsrc(slab_pt, 0, 32768, 0x00020000);
    u32x4 pv[4];
#pragma unroll
    for (int i4 = 0; i4 < 4; ++i4)
      pv[i4] = __builtin_amdgcn_raw_buffer_load_b128(rs, ((wid * 4 + i4) * 64 + lane) * 16, 0, 16);
#pragma unroll
    for (int i4 = 0; i4 < 4; ++i4) {
      f32x4 o = *(LAS f32x4*)(lds + ((wid * 4 + i4) * 64 + lane) * 16) + __builtin_bit_cast(f32x4, pv[i4]);
      o[0] += g * rbf(bf2f(xv[i4][0] & 0xffffu) * bf2f(dv[i4][0] & 0xffffu));
      o[1] += g * rbf(bf2f(xv[i4][0] >> 16) * bf2f(dv[i4][0] >> 16));
      o[2] += g * rbf(bf2f(xv[i4][1] & 0xffffu) * bf2f(dv[i4][1] & 0xffffu));
      o[3] += g * rbf(bf2f(xv[i4][1] >> 16) * bf2f(dv[i4][1] >> 16));
      *(f32x4*)(out + (size_t)t * D_ + d0 + 8 * i4) = o;
    }
  }
}

__global__ __launch_bounds__(512, 2) void k_main(const u16* __restrict__ x, const u16* __restrict__ w1,
                                                 const u16* __restrict__ w3, const u16* __restrict__ bre,
                                                 const u16* __restrict__ bim, char* __restrict__ ws,
                                                 const u16* __restrict__ cre, const u16* __restrict__ cim,
                                                 const u16* __restrict__ w2, const u16* __restrict__ dskip,
                                                 float* __restrict__ out) {
  extern __shared__ __attribute__((aligned(16))) char shm[];
  f32x16 acc2[4];
  phase1(acc2, (LAS char*)shm, x, w1, w3, bre, bim, ws, cre, cim, w2);
  p2_finish(acc2, (LAS char*)shm, x, dskip, ws, out);
  asm volatile("gm_kmain_end:" ::: "memory");
}

extern "C" void kernel_launch(void* const* d_in, const int* in_sizes, int n_in, void* d_out, int out_size,
                              void* d_ws, size_t ws_size, hipStream_t stream) {
  const u16* x = (const u16*)d_in[0];
  const u16* lre = (const u16*)d_in[1];
  const u16* lim = (const u16*)d_in[2];
  const u16* bre = (const u16*)d_in[3];
  const u16* bim = (const u16*)d_in[4];
  const u16* cre = (const u16*)d_in[5];
  const u16* cim = (const u16*)d_in[6];
  const u16* ldt = (const u16*)d_in[7];
  const u16* dsk = (const u16*)d_in[8];
  const u16* w1 = (const u16*)d_in[10];
  const u16* w3 = (const u16*)d_in[11];
  const u16* w2 = (const u16*)d_in[12];
  const u16* gw = (const u16*)d_in[13];
  char* ws = (char*)d_ws;
  k_prep<<<256, 512, 0, stream>>>(x, gw, lre, lim, ldt, ws, w1, w3, bre, bim, cre, cim, w2);
  (void)hipFuncSetAttribute((const void*)k_main, hipFuncAttributeMaxDynamicSharedMemorySize, G1_LDS);
  k_main<<<256, 512, G1_LDS, stream>>>(x, w1, w3, bre, bim, ws, cre, cim, w2, dsk, (float*)d_out);
}
```
